```python
import torch

def _softmax(x, axis=-1):
    return torch.softmax(x, dim=axis)

def _gelu(x):
    return torch.nn.functional.gelu(x, approximate='tanh')

def _silu(x):
    return torch.nn.functional.silu(x)

def _sigmoid(x):
    return torch.sigmoid(x)

def _layernorm(x, axis=-1, eps=1e-05):
    m = x.mean(dim=axis, keepdim=True)
    v = x.var(dim=axis, keepdim=True, unbiased=False)
    return (x - m) / torch.sqrt(v + eps)

def _scatter_add(tgt, idx, src, axis=0):
    return tgt.clone().index_add_(axis, idx, src)

def _segment_sum(src, seg, n):
    out = torch.zeros((n,) + src.shape[1:], dtype=src.dtype)
    return out.index_add_(0, seg, src)

def _pad(x, pw):
    flat = tuple((v for pair in reversed(pw) for v in pair))
    return torch.nn.functional.pad(x, flat)
kernel_name = 'gated_mix__memory_aug_recurrent_mem_tr__swiglu_ffn__prompt'

def setup_inputs():

    _w = lambda a: (lambda t: t.to(torch.bfloat16) if t.is_floating_point() else t)(torch.as_tensor(a))

    def _A_setup():
        B, S, Dm = (1, 2048, 448)
        H, Dh = (16, 256)
        M, seg = (8, 32)
        seg = min(seg, S)
        S = S // seg * seg
        return {'x': _w((lambda *s: torch.randn(*s, dtype=torch.bfloat16) * 0.05)(B, S, Dm)), 'mem0': _w((lambda *s: torch.randn(*s, dtype=torch.bfloat16) * 0.4)(B, M, Dm)), 'Wq': _w((lambda *s: torch.randn(*s, dtype=torch.bfloat16) * 6.4)(Dm, H, Dh) * Dm ** (-0.5)), 'Wk': _w((lambda *s: torch.randn(*s, dtype=torch.bfloat16) * 6.4)(Dm, H, Dh) * Dm ** (-0.5)), 'Wv': _w((lambda *s: torch.randn(*s, dtype=torch.bfloat16) * 0.4)(Dm, H, Dh) * Dm ** (-0.5)), 'Wo': _w((lambda *s: torch.randn(*s, dtype=torch.bfloat16) * 0.4)(H, Dh, Dm) * (H * Dh) ** (-0.5)), 'seg_len': seg}
    _A_in = _A_setup()

    def _B_setup():
        B, S, D, F = (1, 2048, 448, 1792)
        return {'x': _w((lambda *s: torch.randn(*s, dtype=torch.bfloat16) * 0.05)(B, S, D)), 'w_gate': _w((lambda *s: torch.randn(*s, dtype=torch.bfloat16) * 0.05)(D, F)), 'w_up': _w((lambda *s: torch.randn(*s, dtype=torch.bfloat16) * 0.05)(D, F)), 'w_down': _w((lambda *s: torch.randn(*s, dtype=torch.bfloat16) * 0.05)(F, D))}
    _B_in = _B_setup()
    return {'A_x': _A_in['x'], 'A_mem0': _A_in['mem0'], 'A_Wq': _A_in['Wq'], 'A_Wk': _A_in['Wk'], 'A_Wv': _A_in['Wv'], 'A_Wo': _A_in['Wo'], 'A_seg_len': _A_in['seg_len'], 'B_x': _B_in['x'], 'B_w_gate': _B_in['w_gate'], 'B_w_up': _B_in['w_up'], 'B_w_down': _B_in['w_down'], 'gate_w': _w((lambda *s: torch.randn(*s, dtype=torch.bfloat16) * 0.05)(448) * 16.0)}

def reference(A_x, A_mem0, A_Wq, A_Wk, A_Wv, A_Wo, A_seg_len, B_x, B_w_gate, B_w_up, B_w_down, gate_w):

    def _A_ref(x, mem0, Wq, Wk, Wv, Wo, seg_len):
        B, S, Dm = x.shape
        H, Dh = (Wq.shape[1], Wq.shape[2])
        M = mem0.shape[1]
        scale = Dh ** (-0.5)
        n_seg = S // seg_len
        L = M + seg_len
        idx = torch.arange(L)
        mask = (idx[:, None] < M) | (idx[:, None] >= idx[None, :])
        mem = mem0
        outs = []
        for s in range(n_seg):
            xs = x[:, s * seg_len:(s + 1) * seg_len, :].clone()
            h = torch.concatenate([mem, xs], axis=1)
            q = torch.einsum('btd,dhe->bhte', h, Wq)
            k = torch.einsum('btd,dhe->bhte', h, Wk)
            v = torch.einsum('btd,dhe->bhte', h, Wv)
            scores = torch.einsum('bhqe,bhke->bhqk', q, k) * scale
            scores = torch.where(mask[None, None], scores, -1000000000.0)
            p = _softmax(scores, axis=-1)
            o = torch.einsum('bhqk,bhke->bhqe', p, v)
            o = torch.einsum('bhte,hed->btd', o, Wo)
            mem = o[:, :M, :].clone()
            outs.append(o[:, M:, :])
        return torch.concatenate(outs, axis=1)

    def _B_ref(x, w_gate, w_up, w_down):
        g = _silu(x @ w_gate)
        u = x @ w_up
        return g * u @ w_down
    x = A_x
    g = _sigmoid((x * gate_w).sum(-1, keepdims=True))
    a = _A_ref(x, A_mem0, A_Wq, A_Wk, A_Wv, A_Wo, A_seg_len)
    b = _B_ref(x, B_w_gate, B_w_up, B_w_down)
    return g * a + (1 - g) * b
```

```cpp
#include <hip/hip_runtime.h>
#include <stdint.h>

namespace {

typedef __bf16 bf16x2 __attribute__((ext_vector_type(2)));
typedef __bf16 bf16x8 __attribute__((ext_vector_type(8)));
typedef __bf16 bf16x16 __attribute__((ext_vector_type(16)));
typedef short s16x4 __attribute__((ext_vector_type(4)));
typedef float f32x4 __attribute__((ext_vector_type(4)));
typedef unsigned u32x4 __attribute__((ext_vector_type(4)));
typedef unsigned u32x2 __attribute__((ext_vector_type(2)));
#define LAS __attribute__((address_space(3)))
#define GAS __attribute__((address_space(1)))
typedef __amdgpu_buffer_rsrc_t rsrc_t;

constexpr unsigned SENT = 0xFFFFFFFFu;
constexpr int NSEG = 64;
constexpr int DM = 448;
constexpr int AUX_SC1 = 16;

constexpr unsigned SZ_W = 16u * 28 * 14 * 1024;
constexpr unsigned OFF_WK = 0;
constexpr unsigned OFF_WV = OFF_WK + SZ_W;
constexpr unsigned OFF_KX = OFF_WV + SZ_W;
constexpr unsigned KX_S = 16u * 2 * 14 * 1024;
constexpr unsigned OFF_VX = OFF_KX + 64u * KX_S;
constexpr unsigned VX_S = 16u * 28 * 1024;
constexpr unsigned OFF_KM = OFF_VX + 64u * VX_S;
constexpr unsigned KM_S = 16u * 14 * 512;
constexpr unsigned OFF_VM = OFF_KM + 64u * KM_S;
constexpr unsigned VM_S = 16u * 28 * 512;
constexpr unsigned OFF_WD = OFF_VM + 64u * VM_S;
constexpr unsigned OFF_HF = OFF_WD + 28u * 56 * 1024;
constexpr unsigned HF_S = 2u * 112 * 512;
constexpr unsigned OFF_BF = OFF_HF + 64u * HF_S;
constexpr unsigned BF_S = 2u * 28 * 512;
constexpr unsigned OFF_SXX = OFF_BF + 64u * BF_S;
constexpr unsigned SXX_S = 16u * 3 * 1024;
constexpr unsigned OFF_G = OFF_SXX + 64u * SXX_S;
constexpr unsigned OFF_FL = OFF_G + 8192;
constexpr unsigned FL_T1K = OFF_FL;
constexpr unsigned FL_T1V = OFF_FL + 256;
constexpr unsigned FL_F1 = OFF_FL + 512;
constexpr unsigned FL_CH = OFF_FL + 768;
constexpr unsigned FL_T2 = OFF_FL + 1024;
constexpr unsigned FL_F2 = OFF_FL + 1280;
constexpr unsigned FL_POL = OFF_FL + 1536;
constexpr unsigned FL_BYTES = 2048;
constexpr unsigned OFF_DBG = OFF_FL + 4096;
constexpr unsigned OFF_MB = 112u << 20;
constexpr unsigned WS_USED = OFF_MB + (8u << 20);
static_assert(OFF_DBG + (2u << 20) <= OFF_MB, "workspace map");
constexpr unsigned K_SM = 0, K_SX = 32768, K_M = 65536, K_REP = 16384, K_PROBE = 98304;

constexpr unsigned SPIN_MAX = 40000u;
constexpr int LDS_BYTES = 128 * 1024;

struct Params {
  const unsigned short* x;
  const unsigned short* mem0;
  const unsigned short* Wq;
  const unsigned short* Wk;
  const unsigned short* Wv;
  const unsigned short* Wo;
  const unsigned short* Wg;
  const unsigned short* Wu;
  const unsigned short* Wd;
  const unsigned short* gw;
  unsigned short* out;
  unsigned char* ws;
};

#define D_SX 6
#define D_SM 0
#define D_MV 8
#define D_MS 32
#define D_BULK 4

__device__ __forceinline__ unsigned pk(float a, float b) {
  bf16x2 v = {(__bf16)a, (__bf16)b};
  return __builtin_bit_cast(unsigned, v);
}
__device__ __forceinline__ unsigned san(unsigned v) { return min(v, 0xFFFFFFFEu); }
__device__ __forceinline__ float bflo(unsigned v) { return __builtin_bit_cast(float, v << 16); }
__device__ __forceinline__ float bfhi(unsigned v) { return __builtin_bit_cast(float, v & 0xFFFF0000u); }
__device__ __forceinline__ float vmax(float a, float b) { return __builtin_elementwise_maximum(a, b); }
template <int N> __device__ __forceinline__ void delay() {
#pragma unroll 1
  for (int i = 0; i < N; ++i) __builtin_amdgcn_s_sleep(1);
}
__device__ __forceinline__ float max16(float v) {
  unsigned u = __builtin_bit_cast(unsigned, v);
  auto rr = __builtin_amdgcn_permlane16_swap(u, u, false, false);
  const unsigned r0 = rr[0], r1 = rr[1];
  return vmax(__builtin_bit_cast(float, r0), __builtin_bit_cast(float, r1));
}
__device__ __forceinline__ float max32(float v) {
  unsigned u = __builtin_bit_cast(unsigned, v);
  auto rr = __builtin_amdgcn_permlane32_swap(u, u, false, false);
  const unsigned r0 = rr[0], r1 = rr[1];
  return vmax(__builtin_bit_cast(float, r0), __builtin_bit_cast(float, r1));
}
__device__ __forceinline__ float sum16(float v) {
  unsigned u = __builtin_bit_cast(unsigned, v);
  auto rr = __builtin_amdgcn_permlane16_swap(u, u, false, false);
  const unsigned r0 = rr[0], r1 = rr[1];
  return __builtin_bit_cast(float, r0) + __builtin_bit_cast(float, r1);
}
__device__ __forceinline__ float sum32(float v) {
  unsigned u = __builtin_bit_cast(unsigned, v);
  auto rr = __builtin_amdgcn_permlane32_swap(u, u, false, false);
  const unsigned r0 = rr[0], r1 = rr[1];
  return __builtin_bit_cast(float, r0) + __builtin_bit_cast(float, r1);
}
__device__ __forceinline__ bf16x8 as_b8(u32x4 v) { return __builtin_bit_cast(bf16x8, v); }
__device__ __forceinline__ s16x4 as_s4(u32x2 v) { return __builtin_bit_cast(s16x4, v); }
__device__ __forceinline__ bf16x16 cat16(bf16x8 a, bf16x8 b) {
  return __builtin_shufflevector(a, b, 0, 1, 2, 3, 4, 5, 6, 7, 8, 9, 10, 11, 12, 13, 14, 15);
}
__device__ __forceinline__ f32x4 mfma32(bf16x8 a, bf16x8 b, f32x4 c) { return __builtin_amdgcn_mfma_f32_16x16x32_bf16(a, b, c, 0, 0, 0); }
__device__ __forceinline__ f32x4 mfma16(s16x4 a, s16x4 b, f32x4 c) { return __builtin_amdgcn_mfma_f32_16x16x16bf16_1k(a, b, c, 0, 0, 0); }
__device__ __forceinline__ f32x4 smf(bf16x8 a, bf16x16 b, f32x4 c, int idx) { return __builtin_amdgcn_smfmac_f32_16x16x64_bf16(a, b, c, idx, 0, 0); }
__device__ __forceinline__ u32x4 ld16(rsrc_t rs, unsigned v, unsigned s) { return __builtin_amdgcn_raw_buffer_load_b128(rs, v, s, AUX_SC1); }
__device__ __forceinline__ u32x2 ld8(rsrc_t rs, unsigned v, unsigned s) { return __builtin_amdgcn_raw_buffer_load_b64(rs, v, s, AUX_SC1); }
__device__ __forceinline__ unsigned ld4(rsrc_t rs, unsigned v, unsigned s) { return __builtin_amdgcn_raw_buffer_load_b32(rs, v, s, AUX_SC1); }
__device__ __forceinline__ void st16(u32x4 d, rsrc_t rs, unsigned v, unsigned s) { __builtin_amdgcn_raw_buffer_store_b128(d, rs, v, s, AUX_SC1); }
__device__ __forceinline__ void st8(u32x2 d, rsrc_t rs, unsigned v, unsigned s) { __builtin_amdgcn_raw_buffer_store_b64(d, rs, v, s, AUX_SC1); }
__device__ __forceinline__ void st4(unsigned d, rsrc_t rs, unsigned v, unsigned s) { __builtin_amdgcn_raw_buffer_store_b32(d, rs, v, s, AUX_SC1); }
__device__ __forceinline__ rsrc_t ws_rsrc(unsigned char* ws) { return __builtin_amdgcn_make_buffer_rsrc(ws, 0, WS_USED, 0x00020000); }
__device__ __forceinline__ void glds16(const void* src, LAS unsigned char* dst) {
  __builtin_amdgcn_global_load_lds((const GAS unsigned*)src, (LAS unsigned*)dst, 16, 0, 0);
}
#define BAR() do { asm volatile("s_waitcnt lgkmcnt(0)" ::: "memory"); __builtin_amdgcn_s_barrier(); asm volatile("" ::: "memory"); } while (0)
#define VMWAIT0() asm volatile("s_waitcnt vmcnt(0)" ::: "memory")
#define SCHED() __builtin_amdgcn_sched_barrier(0)

__device__ __forceinline__ bool any_sent4(u32x4 v) {
  unsigned m = max(max(v.x, v.y), max(v.z, v.w));
  return __builtin_amdgcn_ballot_w64(m == SENT) != 0ull;
}
__device__ __forceinline__ bool any_sent2(u32x2 a, u32x2 b) {
  unsigned m = max(max(a.x, a.y), max(b.x, b.y));
  return __builtin_amdgcn_ballot_w64(m == SENT) != 0ull;
}
__device__ __forceinline__ unsigned mbase(int s, int pol) {
  return OFF_MB + (unsigned)s * 131072u + (((unsigned)pol >> (s >> 4)) & 1u) * 8192u;
}
__device__ __forceinline__ int wait_flags(rsrc_t rs, unsigned off, int n, int need, int& tmo) {
  const int lane = threadIdx.x & 63;
  const unsigned vo = (unsigned)(lane < n ? lane : 0) * 4u;
  int lo = 0;
  for (unsigned sp = 0;; ++sp) {
    asm volatile("" ::: "memory");
    int v = (int)ld4(rs, vo, off);
    if (__builtin_amdgcn_ballot_w64(v < need) == 0ull) {
      lo = need;
      while (lo < NSEG && __builtin_amdgcn_ballot_w64(v < lo + 1) == 0ull) ++lo;
      break;
    }
    if (sp > SPIN_MAX || tmo) { tmo = 1; lo = NSEG + 1; break; }
    __builtin_amdgcn_s_sleep(4);
  }
  return lo;
}
__device__ __forceinline__ int wait_flags2(rsrc_t rs, unsigned off0, unsigned off1, int need, int& tmo) {
  const int lane = threadIdx.x & 63;
  const unsigned vo = (lane & 1) ? off1 : off0;
  int lo = 0;
  for (unsigned sp = 0;; ++sp) {
    asm volatile("" ::: "memory");
    int v = (int)ld4(rs, vo, 0);
    if (__builtin_amdgcn_ballot_w64(v < need) == 0ull) {
      lo = need;
      while (lo < NSEG && __builtin_amdgcn_ballot_w64(v < lo + 1) == 0ull) ++lo;
      break;
    }
    if (sp > SPIN_MAX || tmo) { tmo = 1; lo = NSEG + 1; break; }
    __builtin_amdgcn_s_sleep(4);
  }
  return lo;
}

__global__ void __launch_bounds__(512, 2) prep_kernel(Params P) {
  extern __shared__ __attribute__((aligned(16))) unsigned char smem[];
  LAS unsigned char* lds = (LAS unsigned char*)smem;
  const int tid = threadIdx.x, lane = tid & 63, wave = __builtin_amdgcn_readfirstlane(tid >> 6);
  const int b = blockIdx.x, xcd = b & 7, j = b >> 3;
  const int wh = xcd * 4 + (j >> 3), which = wh >> 4, h = wh & 15, rb = (j >> 2) & 1, cq = j & 3;
  const int n = lane & 15, g = lane >> 4;
  const rsrc_t rs = ws_rsrc(P.ws);

  if (which == 0) {
#pragma unroll
    for (int k = 0; k < 7; ++k) {
      const int fr = wave * 7 + k;
      const int ct = 7 * cq + (fr >> 3), ke = fr & 7;
      const int dp = 32 * (ct >> 1) + 8 * (n >> 2) + 4 * (ct & 1) + (n & 3);
      glds16(P.Wq + ((size_t)(dp * 16 + h) * 256 + 32 * ke + 8 * g), lds + fr * 1024);
    }
  } else {
#pragma unroll
    for (int k = 0; k < 7; ++k) {
      const int i = (wave * 7 + k) * 64 + lane;
      const int e = i / 14, ch = i - e * 14;
      glds16(P.Wo + ((size_t)(h * 256 + e) * 448 + 112 * cq + 8 * ch), lds + (wave * 7 + k) * 1024);
    }
  }
  bf16x8 a[2][8];
  const int kk = 7 * rb + (wave < 7 ? wave : 0);
  {
    const unsigned short* W1 = which ? P.Wv : P.Wk;
#pragma unroll
    for (int T = 0; T < 2; ++T) {
      const int d = 32 * kk + 8 * (n >> 2) + 4 * T + (n & 3);
      const unsigned short* p = W1 + ((size_t)(d * 16 + h) * 256 + 8 * g);
#pragma unroll
      for (int ke = 0; ke < 8; ++ke) a[T][ke] = *(const bf16x8*)(p + 32 * ke);
    }
  }
  {
    const u32x4 sv = {SENT, SENT, SENT, SENT};
#pragma unroll
    for (int k = 0; k < 4; ++k) {
      const unsigned o = (unsigned)b * 32768u + (unsigned)(k * 512 + tid) * 16u;
      if (!(o >= K_M && o < K_M + 32768u)) st16(sv, rs, OFF_MB + o, 0);
    }
    if (b == 2) {
      for (int i = tid; i < 1792; i += 512) {
        const int ks = i >> 8, gg = (i >> 6) & 3, hi = (i >> 5) & 1, r = (i >> 2) & 7, p = i & 3;
        const unsigned v = san(*(const unsigned*)(P.mem0 + r * DM + 64 * ks + 16 * gg + 4 * p + 2 * hi));
#pragma unroll
        for (int c = 0; c < 4; ++c) st4(v, rs, OFF_MB + K_M + (unsigned)c * 8192u + (unsigned)i * 4u, 0);
      }
    }
    if (b == 3 && tid < (int)(FL_BYTES / 4)) st4(tid == (int)((FL_POL - OFF_FL) / 4) ? SENT : 0u, rs, OFF_FL + (unsigned)tid * 4u, 0);
  }
  if (wave == 7) {
    for (int fr = b; fr < 28 * 56; fr += 256) {
      const int cb = fr / 56, u = fr - cb * 56;
      unsigned short v[8];
#pragma unroll
      for (int jj = 0; jj < 8; ++jj) v[jj] = P.Wd[(size_t)(32 * u + 16 * (jj >> 2) + 4 * g + (jj & 3)) * DM + 16 * cb + n];
      u32x4 o = {v[0] | ((unsigned)v[1] << 16), v[2] | ((unsigned)v[3] << 16), v[4] | ((unsigned)v[5] << 16), v[6] | ((unsigned)v[7] << 16)};
      *(u32x4*)(P.ws + OFF_WD + (size_t)fr * 1024 + lane * 16) = o;
    }
  }
  VMWAIT0();
  __syncthreads();
  if (wave == 7) return;
  f32x4 acc[2][7];
#pragma unroll
  for (int c = 0; c < 7; ++c) { acc[0][c] = f32x4{0, 0, 0, 0}; acc[1][c] = f32x4{0, 0, 0, 0}; }
  if (which == 0) {
#pragma unroll
    for (int ke = 0; ke < 8; ++ke)
#pragma unroll
      for (int c = 0; c < 7; ++c) {
        const bf16x8 bb = *(const LAS bf16x8*)(lds + (c * 8 + ke) * 1024 + lane * 16);
        acc[0][c] = mfma32(a[0][ke], bb, acc[0][c]);
        acc[1][c] = mfma32(a[1][ke], bb, acc[1][c]);
      }
  } else {
    const LAS unsigned char* tb = lds + (8 * g + (n >> 2)) * 224 + (n & 3) * 8;
#pragma unroll
    for (int ke = 0; ke < 8; ++ke)
#pragma unroll
      for (int c = 0; c < 7; ++c) {
        typedef short v4s __attribute__((ext_vector_type(4)));
        const v4s lo = __builtin_amdgcn_ds_read_tr16_b64_v4i16((LAS v4s*)(tb + ke * 32 * 224 + c * 32));
        const v4s hi = __builtin_amdgcn_ds_read_tr16_b64_v4i16((LAS v4s*)(tb + ke * 32 * 224 + 4 * 224 + c * 32));
        const bf16x8 bb = __builtin_bit_cast(bf16x8, __builtin_shufflevector(lo, hi, 0, 1, 2, 3, 4, 5, 6, 7));
        acc[0][c] = mfma32(a[0][ke], bb, acc[0][c]);
        acc[1][c] = mfma32(a[1][ke], bb, acc[1][c]);
      }
  }
  const float sc = which ? 1.0f : 0.0625f * 1.4426950408889634f;
  unsigned char* dst = P.ws + (which ? OFF_WV : OFF_WK) + ((size_t)(h * 28 + 7 * cq) * 14 + kk) * 1024 + lane * 16;
#pragma unroll
  for (int c = 0; c < 7; ++c) {
    const f32x4 u = acc[0][c] * sc, v = acc[1][c] * sc;
    const u32x4 o = {pk(u[0], u[1]), pk(u[2], u[3]), pk(v[0], v[1]), pk(v[2], v[3])};
    *(u32x4*)(dst + (size_t)c * 14 * 1024) = o;
  }
}

__device__ __forceinline__ void gather_m(rsrc_t rs, unsigned voff, unsigned so0, LAS unsigned char* dst, bool act, int& tmo) {
  u32x4 v = ld16(rs, voff, so0);
  if (__builtin_expect(any_sent4(v), 0)) {
    for (unsigned sp = 1;; ++sp) {
      asm volatile("" ::: "memory");
      v = ld16(rs, voff, so0 + ((sp & 1) ? K_REP : 0u));
      if (!any_sent4(v)) break;
      if (sp > SPIN_MAX || tmo) { tmo = 1; break; }
    }
  }
  if (act) *(LAS u32x4*)dst = v;
}

__device__ __forceinline__ int get_pol(rsrc_t rs, int& tmo) {
  unsigned v;
  for (unsigned sp = 0;; ++sp) {
    asm volatile("" ::: "memory");
    v = (unsigned)__builtin_amdgcn_readfirstlane((int)ld4(rs, 0, FL_POL));
    if (v != SENT) break;
    if (sp > SPIN_MAX) { tmo = 1; v = 0; break; }
    __builtin_amdgcn_s_sleep(2);
  }
  return (int)v;
}

__device__ __forceinline__ void probe_pol(const Params& P, rsrc_t rs, int wave, int lane, LAS unsigned char* lds) {
  LAS unsigned* pr = (LAS unsigned*)(lds + 12288);
  unsigned best = 0xFFFFFFFFu;
  const int page = wave >> 1, cls = wave & 1;
#pragma unroll
  for (int r = 0; r < 4; ++r) {
    const unsigned off = OFF_MB + (unsigned)(page * 16 + r) * 131072u + K_PROBE + (unsigned)cls * 8192u + (unsigned)r * 256u;
    const unsigned long long c0 = __builtin_amdgcn_s_memtime();
    unsigned v = ld4(rs, 0, off);
    asm volatile("s_waitcnt vmcnt(0)" : "+v"(v)::"memory");
    const unsigned long long c1 = __builtin_amdgcn_s_memtime();
    best = min(best, (unsigned)(c1 - c0));
  }
  if (lane == 0) pr[wave] = best;
  BAR();
  if (wave == 0 && lane == 0) {
    unsigned pol = 0;
    for (int p = 0; p < 4; ++p) pol |= (pr[2 * p + 1] < pr[2 * p] ? 1u : 0u) << p;
    st4(pol, rs, FL_POL, 0);
  }
}

__device__ __forceinline__ void score_main(const Params& P, int idx, int wave, LAS unsigned char* lds) {
  const int lane = threadIdx.x & 63;
  const int h = idx >> 1, q = idx & 1;
  const rsrc_t rs = ws_rsrc(P.ws);
  LAS unsigned char* img = lds;
  LAS float* red = (LAS float*)(lds + 8192);
  const int g = lane >> 4, n = lane & 15;
  const int idxv = (n & 8) ? 0xEEEE : 0x4444;
  s16x4 E;
#pragma unroll
  for (int jj = 0; jj < 4; ++jj) E[jj] = (((4 * g + jj) & 7) == n) ? (short)0x3F80 : (short)0;
  int tmo = 0;
  bf16x16 w0[7], w1[7];
  const int Pp = 7 * q + wave;
  {
    const unsigned char* b0 = P.ws + OFF_WK + ((size_t)(h * 28 + 2 * Pp) * 14) * 1024 + lane * 16;
#pragma unroll
    for (int ks = 0; ks < 7; ++ks) {
      w0[ks] = cat16(*(const bf16x8*)(b0 + (2 * ks) * 1024), *(const bf16x8*)(b0 + (2 * ks + 1) * 1024));
      w1[ks] = cat16(*(const bf16x8*)(b0 + (14 + 2 * ks) * 1024), *(const bf16x8*)(b0 + (15 + 2 * ks) * 1024));
    }
  }
  if (idx == 0) probe_pol(P, rs, wave, lane, lds);
  const int pol = get_pol(rs, tmo);
  const unsigned gloc = (unsigned)(wave * 56 + (lane < 56 ? lane : 55)) * 16u;
  const unsigned gvoff = gloc + K_M;
  LAS unsigned char* gdst = img + gloc;
  const LAS unsigned char* afp = img + lane * 16;
  const LAS unsigned char* mp = img + (Pp >> 1) * 1024 + (2 * (Pp & 1) + (g >> 1)) * 256 + (n & 7) * 16 + (g & 1) * 8;
  LAS float* redw = red + wave * 64 + n * 8 + 4 * g;
  const unsigned kmv = OFF_KM + (unsigned)(h * 14 + Pp) * 512u + (unsigned)(g * 8 + n) * 16u;
  const unsigned smv = K_SM + (unsigned)idx * 256u + lane * 4u;
  for (int s = 0; s < NSEG; ++s) {
    const unsigned mb = mbase(s, pol);
    if (s > 0) delay<D_MS>();
    gather_m(rs, gvoff, mb, gdst, lane < 56, tmo);
    BAR();
    bf16x8 a[7];
#pragma unroll
    for (int ks = 0; ks < 7; ++ks) a[ks] = *(const LAS bf16x8*)(afp + ks * 1024);
    const u32x2 m0 = *(const LAS u32x2*)mp, m1 = *(const LAS u32x2*)(mp + 128);
    SCHED();
    f32x4 c0 = {0, 0, 0, 0}, c1 = {0, 0, 0, 0};
#pragma unroll
    for (int ks = 0; ks < 7; ++ks) {
      c0 = smf(a[ks], w0[ks], c0, idxv);
      c1 = smf(a[ks], w1[ks], c1, idxv);
    }
    const u32x2 t0 = {pk(c0[0], c0[1]), pk(c0[2], c0[3])}, t1 = {pk(c1[0], c1[1]), pk(c1[2], c1[3])};
    const f32x4 z = {0, 0, 0, 0};
    const f32x4 y0 = mfma16(as_s4(t0), E, z), y1 = mfma16(as_s4(t1), E, z);
    const u32x4 kf = {pk(y0[0], y0[1]), pk(y0[2], y0[3]), pk(y1[0], y1[1]), pk(y1[2], y1[3])};
    const u32x4 mf = {m0.x, m1.x, m0.y, m1.y};
    const f32x4 S = mfma32(as_b8(kf), as_b8(mf), z);
    if (n < 8 && g < 2) *(LAS f32x4*)redw = S;
    BAR();
    if (wave == 0 && s < NSEG - 1) {
      const float v0 = red[lane], v1 = red[64 + lane], v2 = red[128 + lane], v3 = red[192 + lane], v4 = red[256 + lane], v5 = red[320 + lane], v6 = red[384 + lane];
      const float v = ((v0 + v1) + (v2 + v3)) + ((v4 + v5) + v6);
      const unsigned o = san(__builtin_bit_cast(unsigned, v));
      st4(o, rs, smv, mb);
      st4(o, rs, smv + K_REP, mb);
    }
    if (s < NSEG - 1) delay<D_BULK>();
    if (n < 8) st16(kf, rs, kmv, (unsigned)s * KM_S);
  }
  VMWAIT0();
  BAR();
}

__device__ __forceinline__ void score_sx(const Params& P, int idx, LAS unsigned char* lds) {
  const int lane = threadIdx.x & 63;
  const int wave = 7;
  const int h = idx >> 1, q = idx & 1;
  const rsrc_t rs = ws_rsrc(P.ws);
  LAS unsigned char* img = lds;
  const int g = lane >> 4, n = lane & 15;
  const int idxv = (n & 8) ? 0xEEEE : 0x4444;
  s16x4 E;
#pragma unroll
  for (int jj = 0; jj < 4; ++jj) E[jj] = (((4 * g + jj) & 7) == n) ? (short)0x3F80 : (short)0;
  int tmo = 0;
  if (idx == 0) probe_pol(P, rs, wave, lane, lds);
  const int pol = get_pol(rs, tmo);
  const unsigned f0 = FL_T1K + (unsigned)(h * 2) * 4u, f1 = f0 + 4u;
  int known = wait_flags2(rs, f0, f1, 1, tmo);
  u32x4 kx[14];
  const unsigned kxv = OFF_KX + (unsigned)((h * 2 + q) * 14) * 1024u + lane * 16;
#pragma unroll
  for (int k = 0; k < 14; ++k) kx[k] = ld16(rs, kxv + k * 1024, 0);
  const unsigned gloc = (unsigned)(wave * 56 + (lane < 56 ? lane : 55)) * 16u;
  const unsigned gvoff = gloc + K_M;
  LAS unsigned char* gdst = img + gloc;
  const LAS unsigned char* afp = img + lane * 16;
  const unsigned sxv = K_SX + (unsigned)idx * 256u + (unsigned)(g * 8 + (n & 7)) * 8u;
  const unsigned chf = FL_CH + (unsigned)idx * 4u;
  __builtin_amdgcn_s_setprio(3);
  for (int s = 0; s < NSEG; ++s) {
    const unsigned mb = mbase(s, pol);
    if (s > 0) delay<D_MS>();
    gather_m(rs, gvoff, mb, gdst, lane < 56, tmo);
    BAR();
    {
      bf16x8 a[7];
#pragma unroll
      for (int ks = 0; ks < 7; ++ks) a[ks] = *(const LAS bf16x8*)(afp + ks * 1024);
      SCHED();
      f32x4 c0 = {0, 0, 0, 0};
#pragma unroll
      for (int ks = 0; ks < 7; ++ks) c0 = smf(a[ks], cat16(as_b8(kx[2 * ks]), as_b8(kx[2 * ks + 1])), c0, idxv);
      const u32x2 t0 = {pk(c0[0], c0[1]), pk(c0[2], c0[3])};
      const f32x4 z = {0, 0, 0, 0};
      const f32x4 y0 = mfma16(as_s4(t0), E, z);
      const u32x2 o = {san(pk(y0[0], y0[1])), san(pk(y0[2], y0[3]))};
      if (n < 8 && s < NSEG - 1) {
        st8(o, rs, sxv, mb);
        st8(o, rs, sxv + K_REP, mb);
      }
      if (lane == 0) st4((unsigned)s, rs, chf, 0);
      SCHED();
    }
    BAR();
    if (s < NSEG - 1) delay<D_BULK>();
    const int sn = min(s + 1, NSEG - 2);
    if (__builtin_expect(known <= sn, 0)) known = wait_flags2(rs, f0, f1, sn + 1, tmo);
#pragma unroll
    for (int k = 0; k < 14; ++k) kx[k] = ld16(rs, kxv + k * 1024, (unsigned)sn * KX_S);
    SCHED();
  }
  VMWAIT0();
  BAR();
  if (lane == 0) st4((unsigned)NSEG, rs, chf, 0);
}

__device__ __forceinline__ void value_wg(const Params& P, int cb, LAS unsigned char* lds) {
  const int tid = threadIdx.x, lane = tid & 63, wave = __builtin_amdgcn_readfirstlane(tid >> 6);
  const rsrc_t rs = ws_rsrc(P.ws);
  LAS unsigned char* img = lds;
  LAS float* red = (LAS float*)(lds + 8192);
  const int g = lane >> 4, n = lane & 15, sel = n >> 3, i = n & 7;
  const int idxv = (n & 8) ? 0xEEEE : 0x4444;
  const int h0 = 2 * wave, hs = h0 + sel;
  int tmo = 0;
  bf16x16 w0[7], w1[7];
  {
    const unsigned char* b0 = P.ws + OFF_WV + ((size_t)(h0 * 28 + cb) * 14) * 1024 + lane * 16;
    const unsigned char* b1 = b0 + (size_t)28 * 14 * 1024;
#pragma unroll
    for (int ks = 0; ks < 7; ++ks) {
      w0[ks] = cat16(*(const bf16x8*)(b0 + (2 * ks) * 1024), *(const bf16x8*)(b0 + (2 * ks + 1) * 1024));
      w1[ks] = cat16(*(const bf16x8*)(b1 + (2 * ks) * 1024), *(const bf16x8*)(b1 + (2 * ks + 1) * 1024));
    }
  }
  const int pol = get_pol(rs, tmo);
  const unsigned fa = FL_T1V + (unsigned)(h0 * 2 + (cb >= 14 ? 1 : 0)) * 4u, fb = fa + 8u;
  int known = 0;
  const unsigned vxv = OFF_VX + (unsigned)(h0 * 28 + cb) * 1024u + lane * 16;
  const unsigned gloc = (unsigned)(wave * 56 + (lane < 56 ? lane : 55)) * 16u;
  const unsigned gvoff = gloc + K_M;
  LAS unsigned char* gdst = img + gloc;
  const LAS unsigned char* afp = img + lane * 16;
  const unsigned vmv = OFF_VM + (unsigned)(h0 * 28 + cb) * 512u + lane * 8;
  const unsigned sxv = K_SX + (unsigned)(hs * 2) * 256u + (unsigned)(g * 8 + i) * 8u;
  const unsigned smv = K_SM + (unsigned)(hs * 2) * 256u + (unsigned)(i * 8 + 4 * (g & 1)) * 4u;
  LAS float* redw = red + wave * 128 + i * 16 + 4 * g;
  const LAS float* redr = red + ((lane >> 2) & 7) * 16 + 4 * (lane & 3) + 2 * (lane >> 5);
  const unsigned mv = K_M + (unsigned)cb * 256u + lane * 4u;
  const unsigned chf = FL_CH + (unsigned)(32 + cb) * 4u;
  u32x2 vm0 = {0, 0}, vm1 = {0, 0};
  for (int s = 0; s < NSEG - 1; ++s) {
    const unsigned mb = mbase(s, pol), mbn = mbase(s + 1, pol);
    if (s > 0) delay<D_MV>();
    gather_m(rs, gvoff, mb, gdst, lane < 56, tmo);
    BAR();
    if (__builtin_expect(known <= s, 0)) known = wait_flags2(rs, fa, fb, s + 1, tmo);
    bf16x8 a[7];
#pragma unroll
    for (int ks = 0; ks < 7; ++ks) a[ks] = *(const LAS bf16x8*)(afp + ks * 1024);
    const u32x4 vx0 = ld16(rs, vxv, (unsigned)s * VX_S);
    const u32x4 vx1 = ld16(rs, vxv + 28 * 1024, (unsigned)s * VX_S);
    st8(vm0, rs, vmv, (unsigned)max(s - 1, 0) * VM_S);
    st8(vm1, rs, vmv + 28 * 512, (unsigned)max(s - 1, 0) * VM_S);
    SCHED();
    f32x4 c0 = {0, 0, 0, 0}, c1 = {0, 0, 0, 0};
#pragma unroll
    for (int ks = 0; ks < 7; ++ks) {
      c0 = smf(a[ks], w0[ks], c0, idxv);
      c1 = smf(a[ks], w1[ks], c1, idxv);
    }
    vm0 = u32x2{pk(c0[0], c0[1]), pk(c0[2], c0[3])};
    vm1 = u32x2{pk(c1[0], c1[1]), pk(c1[2], c1[3])};
    SCHED();
    delay<D_SX>();
    u32x2 x0 = ld8(rs, sxv, mb), x1 = ld8(rs, sxv + 256, mb);
    if (__builtin_expect(any_sent2(x0, x1), 0)) {
      for (unsigned sp = 1;; ++sp) {
        asm volatile("" ::: "memory");
        const unsigned so = mb + ((sp & 1) ? K_REP : 0u);
        x0 = ld8(rs, sxv, so);
        x1 = ld8(rs, sxv + 256, so);
        if (!any_sent2(x0, x1)) break;
        if (sp > SPIN_MAX || tmo) { tmo = 1; break; }
      }
    }
    delay<D_SM>();
    u32x4 ua = ld16(rs, smv, mb), ub = ld16(rs, smv + 256, mb);
    SCHED();
    float t[8] = {bflo(x0.x), bfhi(x0.x), bflo(x0.y), bfhi(x0.y), bflo(x1.x), bfhi(x1.x), bflo(x1.y), bfhi(x1.y)};
    float m1 = vmax(vmax(vmax(t[0], t[1]), vmax(t[2], t[3])), vmax(vmax(t[4], t[5]), vmax(t[6], t[7])));
    m1 = max32(max16(m1));
    float l1 = 0.f;
#pragma unroll
    for (int k = 0; k < 8; ++k) { t[k] = __builtin_amdgcn_exp2f(t[k] - m1); l1 += t[k]; }
    l1 = sum32(sum16(l1));
    const u32x4 pb = {pk(t[0], t[1]), pk(t[2], t[3]), pk(t[4], t[5]), pk(t[6], t[7])};
    const f32x4 z = {0, 0, 0, 0};
    const f32x4 ax0 = mfma32(as_b8(vx0), as_b8(pb), z), ax1 = mfma32(as_b8(vx1), as_b8(pb), z);
    const f32x4 ax = sel ? ax1 : ax0;
    SCHED();
    if (__builtin_expect(any_sent4(ua) || any_sent4(ub), 0)) {
      for (unsigned sp = 1;; ++sp) {
        asm volatile("" ::: "memory");
        const unsigned so = mb + ((sp & 1) ? K_REP : 0u);
        ua = ld16(rs, smv, so);
        ub = ld16(rs, smv + 256, so);
        if (!(any_sent4(ua) || any_sent4(ub))) break;
        if (sp > SPIN_MAX || tmo) { tmo = 1; break; }
      }
    }
    const f32x4 sm = __builtin_bit_cast(f32x4, ua) + __builtin_bit_cast(f32x4, ub);
    float m2 = vmax(vmax(sm[0], sm[1]), vmax(sm[2], sm[3]));
    m2 = max16(m2);
    const float M = vmax(m1, m2);
    const float al = __builtin_amdgcn_exp2f(m1 - M);
    const float e0 = __builtin_amdgcn_exp2f(sm[0] - M), e1 = __builtin_amdgcn_exp2f(sm[1] - M), e2 = __builtin_amdgcn_exp2f(sm[2] - M), e3 = __builtin_amdgcn_exp2f(sm[3] - M);
    const u32x2 pm = {pk(e0, e1), pk(e2, e3)};
    const f32x4 cx = ax * al;
    const f32x4 r0 = mfma16(as_s4(vm0), as_s4(pm), cx), r1 = mfma16(as_s4(vm1), as_s4(pm), cx);
    const float l2 = sum16((e0 + e1) + (e2 + e3));
    const float rl = __builtin_amdgcn_rcpf(al * l1 + l2);
    f32x4 r = (sel ? r1 : r0) * rl;
#pragma unroll
    for (int k = 0; k < 4; ++k) {
      const float rk = r[k];
      const int o = __builtin_amdgcn_update_dpp(0, __builtin_bit_cast(int, rk), 0x128, 0xf, 0xf, true);
      r[k] += __builtin_bit_cast(float, o);
    }
    if (sel == 0) *(LAS f32x4*)redw = r;
    BAR();
    if (wave == 0) {
      float a0[8], a1[8];
#pragma unroll
      for (int w = 0; w < 8; ++w) { a0[w] = redr[w * 128]; a1[w] = redr[w * 128 + 1]; }
      const float s0 = ((a0[0] + a0[1]) + (a0[2] + a0[3])) + ((a0[4] + a0[5]) + (a0[6] + a0[7]));
      const float s1 = ((a1[0] + a1[1]) + (a1[2] + a1[3])) + ((a1[4] + a1[5]) + (a1[6] + a1[7]));
      const unsigned o = san(pk(s0, s1));
      st4(o, rs, mv, mbn);
      st4(o, rs, mv + K_REP, mbn);
    }
    if (wave == 1 && lane == 0) st4((unsigned)s, rs, chf, 0);
  }
  {
    const int s = NSEG - 1;
    const unsigned mb = mbase(s, pol);
    delay<D_MV>();
    gather_m(rs, gvoff, mb, gdst, lane < 56, tmo);
    BAR();
    st8(vm0, rs, vmv, (unsigned)(s - 1) * VM_S);
    st8(vm1, rs, vmv + 28 * 512, (unsigned)(s - 1) * VM_S);
    f32x4 c0 = {0, 0, 0, 0}, c1 = {0, 0, 0, 0};
#pragma unroll
    for (int ks = 0; ks < 7; ++ks) {
      const bf16x8 a = *(const LAS bf16x8*)(afp + ks * 1024);
      c0 = smf(a, w0[ks], c0, idxv);
      c1 = smf(a, w1[ks], c1, idxv);
    }
    vm0 = u32x2{pk(c0[0], c0[1]), pk(c0[2], c0[3])};
    vm1 = u32x2{pk(c1[0], c1[1]), pk(c1[2], c1[3])};
    st8(vm0, rs, vmv, (unsigned)s * VM_S);
    st8(vm1, rs, vmv + 28 * 512, (unsigned)s * VM_S);
    VMWAIT0();
    BAR();
    if (wave == 1 && lane == 0) st4((unsigned)NSEG, rs, chf, 0);
  }
}

__device__ __forceinline__ void stage_x(const Params& P, int s, LAS unsigned char* buf, int lane, int first, int count) {
  const unsigned short* p = P.x + (size_t)(32 * s + (lane & 15)) * DM + 8 * (lane >> 4);
  for (int k = first; k < first + count; ++k) {
    const int q = k >= 14 ? 1 : 0, kk = k - 14 * q;
    glds16(p + q * 16 * DM + 32 * kk, buf + k * 1024);
  }
}

template <int MODE>
__device__ __forceinline__ void stream_wg(const Params& P, int id, LAS unsigned char* lds) {
  const int tid = threadIdx.x, lane = tid & 63, wave = __builtin_amdgcn_readfirstlane(tid >> 6);
  const rsrc_t rs = ws_rsrc(P.ws);
  const int n = lane & 15, g = lane >> 4;
  bf16x8 w0[14], w1[14];
  if (MODE == 2) {
#pragma unroll 1
    for (int m = 0; m < 2; ++m) {
      const unsigned short* W = m ? P.Wu : P.Wg;
      for (int k = wave; k < 98; k += 8) {
        const int i = k * 64 + lane, d = i / 14, ch = i - d * 14;
        glds16(W + (size_t)d * 1792 + 112 * id + 8 * ch, lds + k * 1024);
      }
      VMWAIT0();
      __syncthreads();
      if (wave < 7) {
        const LAS unsigned char* tb = lds + (8 * g + (n >> 2)) * 224 + (n & 3) * 8 + wave * 32;
#pragma unroll
        for (int kk = 0; kk < 14; ++kk) {
          typedef short v4s __attribute__((ext_vector_type(4)));
          const v4s lo = __builtin_amdgcn_ds_read_tr16_b64_v4i16((LAS v4s*)(tb + kk * 32 * 224));
          const v4s hi = __builtin_amdgcn_ds_read_tr16_b64_v4i16((LAS v4s*)(tb + kk * 32 * 224 + 4 * 224));
          const bf16x8 f = __builtin_bit_cast(bf16x8, __builtin_shufflevector(lo, hi, 0, 1, 2, 3, 4, 5, 6, 7));
          if (m == 0) w0[kk] = f; else w1[kk] = f;
        }
      }
      __syncthreads();
    }
  } else if (wave < 7) {
    const int h = id >> 1, hq = id & 1;
    const unsigned char* b0 = P.ws + (MODE ? OFF_WV : OFF_WK) + ((size_t)(h * 28 + 14 * hq + 2 * wave) * 14) * 1024 + lane * 16;
#pragma unroll
    for (int kk = 0; kk < 14; ++kk) { w0[kk] = *(const bf16x8*)(b0 + kk * 1024); w1[kk] = *(const bf16x8*)(b0 + (14 + kk) * 1024); }
  }
  const unsigned fl = (MODE == 0 ? FL_T1K : MODE == 1 ? FL_T1V : FL_F1) + (unsigned)id * 4u;
  if (wave == 7) {
    stage_x(P, 0, lds, lane, 0, 28);
    VMWAIT0();
    for (int s = 0; s < NSEG; ++s) {
      BAR();
      if (s > 0 && lane == 0) st4((unsigned)s, rs, fl, 0);
      if (s + 1 < NSEG) stage_x(P, s + 1, lds + ((s + 1) & 1) * 28672, lane, 0, 28);
      VMWAIT0();
    }
    BAR();
    if (lane == 0) st4((unsigned)NSEG, rs, fl, 0);
  } else {
    for (int s = 0; s < NSEG; ++s) {
      BAR();
      const LAS unsigned char* xb = lds + (s & 1) * 28672 + lane * 16;
      u32x2 pa[2], pb[2];
#pragma unroll
      for (int q = 0; q < 2; ++q) {
        f32x4 c0 = {0, 0, 0, 0}, c1 = {0, 0, 0, 0};
#pragma unroll
        for (int kk = 0; kk < 14; ++kk) {
          const bf16x8 xf = *(const LAS bf16x8*)(xb + (q * 14 + kk) * 1024);
          if (MODE == 1) { c0 = mfma32(xf, w0[kk], c0); c1 = mfma32(xf, w1[kk], c1); }
          else { c0 = mfma32(w0[kk], xf, c0); c1 = mfma32(w1[kk], xf, c1); }
        }
        if (MODE == 2) {
          float hv[4];
#pragma unroll
          for (int k = 0; k < 4; ++k) {
            const float gg = c0[k];
            hv[k] = gg * __builtin_amdgcn_rcpf(1.0f + __builtin_amdgcn_exp2f(-1.4426950408889634f * gg)) * c1[k];
          }
          const u32x2 o = {pk(hv[0], hv[1]), pk(hv[2], hv[3])};
          st8(o, rs, OFF_HF + (unsigned)(q * 112 + 7 * id + wave) * 512u + lane * 8, (unsigned)s * HF_S);
        } else {
          pa[q] = u32x2{pk(c0[0], c0[1]), pk(c0[2], c0[3])};
          pb[q] = u32x2{pk(c1[0], c1[1]), pk(c1[2], c1[3])};
          if (MODE == 0) {
            const int h = id >> 1, Pp = 7 * (id & 1) + wave;
            const u32x4 o = {pa[q].x, pa[q].y, pb[q].x, pb[q].y};
            st16(o, rs, OFF_KX + (unsigned)((h * 2 + q) * 14 + Pp) * 1024u + lane * 16, (unsigned)s * KX_S);
          }
        }
      }
      if (MODE == 1) {
        const int h = id >> 1, cb = 14 * (id & 1) + 2 * wave;
        const u32x4 o0 = {pa[0].x, pa[0].y, pa[1].x, pa[1].y}, o1 = {pb[0].x, pb[0].y, pb[1].x, pb[1].y};
        st16(o0, rs, OFF_VX + (unsigned)(h * 28 + cb) * 1024u + lane * 16, (unsigned)s * VX_S);
        st16(o1, rs, OFF_VX + (unsigned)(h * 28 + cb + 1) * 1024u + lane * 16, (unsigned)s * VX_S);
      }
      VMWAIT0();
    }
    BAR();
  }
}

__device__ __forceinline__ void task_sxx(const Params& P, int s, LAS unsigned char* lds, int& tmo) {
  const int tid = threadIdx.x, lane = tid & 63, wave = __builtin_amdgcn_readfirstlane(tid >> 6);
  const rsrc_t rs = ws_rsrc(P.ws);
  __syncthreads();
  stage_x(P, s, lds, lane, wave * 4, wave < 7 ? 4 : 0);
  wait_flags(rs, FL_T1K, 32, s + 1, tmo);
  VMWAIT0();
  __syncthreads();
  const LAS unsigned char* xb = lds + lane * 16;
#pragma unroll 1
  for (int hh = 0; hh < 2; ++hh) {
    const int h = 2 * wave + hh;
    const unsigned kv = OFF_KX + (unsigned)(h * 2 * 14) * 1024u + lane * 16;
    u32x4 k0[14], k1[14];
#pragma unroll
    for (int kk = 0; kk < 14; ++kk) { k0[kk] = ld16(rs, kv + kk * 1024, (unsigned)s * KX_S); k1[kk] = ld16(rs, kv + (14 + kk) * 1024, (unsigned)s * KX_S); }
    f32x4 c00 = {0, 0, 0, 0}, c01 = {0, 0, 0, 0}, c11 = {0, 0, 0, 0};
#pragma unroll
    for (int kk = 0; kk < 14; ++kk) {
      const bf16x8 x0 = *(const LAS bf16x8*)(xb + kk * 1024), x1 = *(const LAS bf16x8*)(xb + (14 + kk) * 1024);
      c00 = mfma32(as_b8(k0[kk]), x0, c00);
      c01 = mfma32(as_b8(k0[kk]), x1, c01);
      c11 = mfma32(as_b8(k1[kk]), x1, c11);
    }
    const unsigned o = OFF_SXX + (unsigned)(h * 3) * 1024u + lane * 16;
    st16(__builtin_bit_cast(u32x4, c00), rs, o, (unsigned)s * SXX_S);
    st16(__builtin_bit_cast(u32x4, c01), rs, o + 1024, (unsigned)s * SXX_S);
    st16(__builtin_bit_cast(u32x4, c11), rs, o + 2048, (unsigned)s * SXX_S);
  }
  VMWAIT0();
  __syncthreads();
  if (tid == 0) st4(1u, rs, FL_T2 + (unsigned)s * 4u, 0);
}

__device__ __forceinline__ void task_ffn2(const Params& P, int s, LAS unsigned char* lds, int& tmo) {
  const int tid = threadIdx.x, lane = tid & 63, wave = __builtin_amdgcn_readfirstlane(tid >> 6);
  const rsrc_t rs = ws_rsrc(P.ws);
  __syncthreads();
  wait_flags(rs, FL_F1, 16, s + 1, tmo);
  {
    u32x4 v[14];
#pragma unroll
    for (int k = 0; k < 14; ++k) v[k] = ld16(rs, OFF_HF + (unsigned)(wave * 14 + k) * 1024u + lane * 16, (unsigned)s * HF_S);
#pragma unroll
    for (int k = 0; k < 14; ++k) *(LAS u32x4*)(lds + (wave * 14 + k) * 1024 + lane * 16) = v[k];
  }
  __syncthreads();
  if (wave < 7) {
    f32x4 acc[4][2];
#pragma unroll
    for (int c = 0; c < 4; ++c) { acc[c][0] = f32x4{0, 0, 0, 0}; acc[c][1] = f32x4{0, 0, 0, 0}; }
    const unsigned char* wd = P.ws + OFF_WD + (size_t)(4 * wave) * 56 * 1024 + lane * 16;
    const LAS unsigned char* hb = lds + lane * 8;
#pragma unroll 1
    for (int u0 = 0; u0 < 56; u0 += 4) {
      bf16x8 wf[4][4];
#pragma unroll
      for (int uu = 0; uu < 4; ++uu)
#pragma unroll
        for (int c = 0; c < 4; ++c) wf[uu][c] = *(const bf16x8*)(wd + (size_t)(c * 56 + u0 + uu) * 1024);
#pragma unroll
      for (int uu = 0; uu < 4; ++uu) {
        const int u = u0 + uu;
#pragma unroll
        for (int q = 0; q < 2; ++q) {
          const u32x2 lo = *(const LAS u32x2*)(hb + (q * 112 + 2 * u) * 512), hi = *(const LAS u32x2*)(hb + (q * 112 + 2 * u + 1) * 512);
          const u32x4 hf = {lo.x, lo.y, hi.x, hi.y};
#pragma unroll
          for (int c = 0; c < 4; ++c) acc[c][q] = mfma32(wf[uu][c], as_b8(hf), acc[c][q]);
        }
      }
    }
#pragma unroll
    for (int c = 0; c < 4; ++c)
#pragma unroll
      for (int q = 0; q < 2; ++q) {
        const u32x2 o = {pk(acc[c][q][0], acc[c][q][1]), pk(acc[c][q][2], acc[c][q][3])};
        st8(o, rs, OFF_BF + (unsigned)(q * 28 + 4 * wave + c) * 512u + lane * 8, (unsigned)s * BF_S);
      }
  } else {
    const int t = lane & 31, half = lane >> 5;
    const unsigned short* xp = P.x + (size_t)(32 * s + t) * DM + 224 * half;
    const unsigned short* gp = P.gw + 224 * half;
    float acc = 0.f;
#pragma unroll 4
    for (int k = 0; k < 28; ++k) {
      const u32x4 xv = *(const u32x4*)(xp + 8 * k), gv = *(const u32x4*)(gp + 8 * k);
      acc += bflo(xv.x) * bflo(gv.x) + bfhi(xv.x) * bfhi(gv.x) + bflo(xv.y) * bflo(gv.y) + bfhi(xv.y) * bfhi(gv.y) +
             bflo(xv.z) * bflo(gv.z) + bfhi(xv.z) * bfhi(gv.z) + bflo(xv.w) * bflo(gv.w) + bfhi(xv.w) * bfhi(gv.w);
    }
    acc = sum32(acc);
    const float gate = __builtin_amdgcn_rcpf(1.0f + __builtin_amdgcn_exp2f(-1.4426950408889634f * acc));
    if (lane < 32) st4(__builtin_bit_cast(unsigned, gate), rs, OFF_G + (unsigned)(32 * s + t) * 4u, 0);
  }
  VMWAIT0();
  __syncthreads();
  if (tid == 0) st4(1u, rs, FL_F2 + (unsigned)s * 4u, 0);
}

__device__ __forceinline__ void task_attn(const Params& P, int s, int cbg, LAS unsigned char* lds, int& tmo) {
  const int tid = threadIdx.x, lane = tid & 63, wave = __builtin_amdgcn_readfirstlane(tid >> 6);
  const rsrc_t rs = ws_rsrc(P.ws);
  const int n = lane & 15, g = lane >> 4;
  LAS unsigned char* PT = lds + 28672;
  LAS unsigned char* PM = PT + 32768;
  __syncthreads();
  stage_x(P, s, lds, lane, wave * 4, wave < 7 ? 4 : 0);
  wait_flags2(rs, FL_T2 + (unsigned)s * 4u, FL_F2 + (unsigned)s * 4u, 1, tmo);
  wait_flags(rs, FL_T1V, 32, s + 1, tmo);
  const int cb = 4 * cbg + (wave >> 1), qo = wave & 1;
  u32x4 vx[16];
#pragma unroll
  for (int h = 0; h < 16; ++h) vx[h] = ld16(rs, OFF_VX + (unsigned)(h * 28 + cb) * 1024u + lane * 16, (unsigned)s * VX_S);
  const u32x2 bf = ld8(rs, OFF_BF + (unsigned)(qo * 28 + cb) * 512u + lane * 8, (unsigned)s * BF_S);
  const float gate = __builtin_bit_cast(float, ld4(rs, OFF_G + (unsigned)(32 * s + 16 * qo + n) * 4u, 0));
  VMWAIT0();
  __syncthreads();
  wait_flags(rs, FL_CH, 60, s + 1, tmo);
  u32x2 vm[16];
#pragma unroll
  for (int h = 0; h < 16; ++h) vm[h] = ld8(rs, OFF_VM + (unsigned)(h * 28 + cb) * 512u + lane * 8, (unsigned)s * VM_S);
  const LAS unsigned char* xb = lds + lane * 16;
#pragma unroll 1
  for (int hh = 0; hh < 2; ++hh) {
    const int h = 2 * wave + hh;
    u32x4 km[14];
#pragma unroll
    for (int kk = 0; kk < 14; ++kk) km[kk] = ld16(rs, OFF_KM + (unsigned)(h * 14 + kk) * 512u + (unsigned)(g * 8 + (n & 7)) * 16u, (unsigned)s * KM_S);
    f32x4 sxx[3];
#pragma unroll
    for (int k = 0; k < 3; ++k) sxx[k] = __builtin_bit_cast(f32x4, ld16(rs, OFF_SXX + (unsigned)(h * 3 + k) * 1024u + lane * 16, (unsigned)s * SXX_S));
    f32x4 s0 = {0, 0, 0, 0}, s1 = {0, 0, 0, 0};
#pragma unroll
    for (int kk = 0; kk < 14; ++kk) {
      s0 = mfma32(as_b8(km[kk]), *(const LAS bf16x8*)(xb + kk * 1024), s0);
      s1 = mfma32(as_b8(km[kk]), *(const LAS bf16x8*)(xb + (14 + kk) * 1024), s1);
    }
#pragma unroll
    for (int q = 0; q < 2; ++q) {
      const f32x4 sm = q ? s1 : s0;
      const f32x4 ta = q ? sxx[1] : sxx[0];
      const f32x4 tb = sxx[2];
      float t[8];
      const float NEG = -3.0e38f;
#pragma unroll
      for (int k = 0; k < 4; ++k) {
        const bool causal = (4 * g + k) <= n;
        t[k] = q ? ta[k] : (causal ? ta[k] : NEG);
        t[4 + k] = q ? (causal ? tb[k] : NEG) : NEG;
      }
      float m = vmax(vmax(vmax(sm[0], sm[1]), vmax(sm[2], sm[3])), vmax(vmax(vmax(t[0], t[1]), vmax(t[2], t[3])), vmax(vmax(t[4], t[5]), vmax(t[6], t[7]))));
      m = max32(max16(m));
      float e[4], l = 0.f;
#pragma unroll
      for (int k = 0; k < 4; ++k) { e[k] = __builtin_amdgcn_exp2f(sm[k] - m); l += e[k]; }
      l *= 0.5f;
#pragma unroll
      for (int k = 0; k < 8; ++k) { t[k] = __builtin_amdgcn_exp2f(t[k] - m); l += t[k]; }
      l = sum32(sum16(l));
      const float rl = __builtin_amdgcn_rcpf(l);
      const u32x4 pt = {pk(t[0] * rl, t[1] * rl), pk(t[2] * rl, t[3] * rl), pk(t[4] * rl, t[5] * rl), pk(t[6] * rl, t[7] * rl)};
      const u32x2 pm = {pk(e[0] * rl, e[1] * rl), pk(e[2] * rl, e[3] * rl)};
      *(LAS u32x4*)(PT + (h * 2 + q) * 1024 + lane * 16) = pt;
      *(LAS u32x2*)(PM + (h * 2 + q) * 512 + lane * 8) = pm;
    }
  }
  __syncthreads();
  f32x4 acc = {0, 0, 0, 0}, accm = {0, 0, 0, 0};
#pragma unroll
  for (int h = 0; h < 16; ++h) {
    const bf16x8 pt = *(const LAS bf16x8*)(PT + (h * 2 + qo) * 1024 + lane * 16);
    const u32x2 pm = *(const LAS u32x2*)(PM + (h * 2 + qo) * 512 + lane * 8);
    acc = mfma32(as_b8(vx[h]), pt, acc);
    accm = mfma16(as_s4(vm[h]), as_s4(pm), accm);
  }
  acc += accm;
  const float gb = bflo(pk(gate, 0.f));
  const float og = bflo(pk(1.0f - gb, 0.f));
  const float bv[4] = {bflo(bf.x), bfhi(bf.x), bflo(bf.y), bfhi(bf.y)};
  float o[4];
#pragma unroll
  for (int k = 0; k < 4; ++k) {
    const float av = bflo(pk(acc[k], 0.f));
    o[k] = bflo(pk(gb * av, 0.f)) + bflo(pk(og * bv[k], 0.f));
  }
  const u32x2 ov = {pk(o[0], o[1]), pk(o[2], o[3])};
  *(u32x2*)(P.out + (size_t)(32 * s + 16 * qo + n) * DM + 16 * cb + 4 * g) = ov;
}

constexpr int NGEN = 48;
constexpr int LOOKAHEAD = 8;
__device__ __forceinline__ void generic_wg(const Params& P, int id, LAS unsigned char* lds) {
  int tmo = 0;
  constexpr int NT = NSEG * 9;
  for (int t = id; t < NT; t += NGEN) {
    int kind, s, c = 0;
    if (t < 2 * LOOKAHEAD) { kind = t & 1; s = t >> 1; }
    else {
      const int r = t - 2 * LOOKAHEAD;
      const int full = (NSEG - LOOKAHEAD) * 9;
      if (r < full) {
        const int blk = r / 9, o = r - blk * 9;
        if (o < 7) { kind = 2; s = blk; c = o; } else { kind = o - 7; s = blk + LOOKAHEAD; }
      } else {
        const int r2 = r - full, blk = r2 / 7;
        kind = 2; s = NSEG - LOOKAHEAD + blk; c = r2 - blk * 7;
      }
    }
    if (kind == 0) task_sxx(P, s, lds, tmo);
    else if (kind == 1) task_ffn2(P, s, lds, tmo);
    else task_attn(P, s, c, lds, tmo);
  }
}

__global__ void __launch_bounds__(512, 2) main_kernel(Params P) {
  extern __shared__ __attribute__((aligned(16))) unsigned char smem[];
  LAS unsigned char* lds = (LAS unsigned char*)smem;
  const int b = blockIdx.x, x = b & 7, r = b >> 3;
  if (x < 4) {
    if (x == 0 || x == 2) {
      if (r < 16) {
        const int idx = (x >> 1) * 16 + r;
        const int wave = __builtin_amdgcn_readfirstlane(threadIdx.x >> 6);
        if (wave < 7) score_main(P, idx, wave, lds); else score_sx(P, idx, lds);
      }
    } else if (r < 14) {
      value_wg(P, (x >> 1) * 14 + r, lds);
    }
  } else {
    const int y = x - 4;
    if (r < 8) stream_wg<0>(P, y * 8 + r, lds);
    else if (r < 16) stream_wg<1>(P, y * 8 + r - 8, lds);
    else if (r < 20) stream_wg<2>(P, y * 4 + r - 16, lds);
    else generic_wg(P, y * 12 + r - 20, lds);
  }
}

}

extern "C" void kernel_launch(void* const* d_in, const int* in_sizes, int n_in, void* d_out, int out_size, void* d_ws, size_t ws_size,
                              hipStream_t stream) {
  Params P;
  P.x = (const unsigned short*)d_in[0];
  P.mem0 = (const unsigned short*)d_in[1];
  P.Wq = (const unsigned short*)d_in[2];
  P.Wk = (const unsigned short*)d_in[3];
  P.Wv = (const unsigned short*)d_in[4];
  P.Wo = (const unsigned short*)d_in[5];
  P.Wg = (const unsigned short*)d_in[8];
  P.Wu = (const unsigned short*)d_in[9];
  P.Wd = (const unsigned short*)d_in[10];
  P.gw = (const unsigned short*)d_in[11];
  P.out = (unsigned short*)d_out;
  const uintptr_t base = ((uintptr_t)d_ws + ((1u << 21) - 1)) & ~(uintptr_t)((1u << 21) - 1);
  P.ws = (unsigned char*)base;
  (void)hipFuncSetAttribute((const void*)prep_kernel, hipFuncAttributeMaxDynamicSharedMemorySize, 57344);
  (void)hipFuncSetAttribute((const void*)main_kernel, hipFuncAttributeMaxDynamicSharedMemorySize, LDS_BYTES);
  hipLaunchKernelGGL(prep_kernel, dim3(256), dim3(512), 57344, stream, P);
  hipLaunchKernelGGL(main_kernel, dim3(256), dim3(512), LDS_BYTES, stream, P);
}
```

```cpp
#include <hip/hip_runtime.h>
#include <stdint.h>

namespace {

typedef __bf16 bf16x2 __attribute__((ext_vector_type(2)));
typedef __bf16 bf16x8 __attribute__((ext_vector_type(8)));
typedef __bf16 bf16x16 __attribute__((ext_vector_type(16)));
typedef short s16x4 __attribute__((ext_vector_type(4)));
typedef float f32x4 __attribute__((ext_vector_type(4)));
typedef unsigned u32x4 __attribute__((ext_vector_type(4)));
typedef unsigned u32x2 __attribute__((ext_vector_type(2)));
#define LAS __attribute__((address_space(3)))
#define GAS __attribute__((address_space(1)))
typedef __amdgpu_buffer_rsrc_t rsrc_t;

constexpr unsigned SENT = 0xFFFFFFFFu;
constexpr int NSEG = 64;
constexpr int DM = 448;
constexpr int AUX_SC1 = 16;

constexpr unsigned SZ_W = 16u * 28 * 14 * 1024;
constexpr unsigned OFF_WK = 0;
constexpr unsigned OFF_WV = OFF_WK + SZ_W;
constexpr unsigned OFF_KX = OFF_WV + SZ_W;
constexpr unsigned KX_S = 16u * 2 * 14 * 1024;
constexpr unsigned OFF_VX = OFF_KX + 64u * KX_S;
constexpr unsigned VX_S = 16u * 28 * 1024;
constexpr unsigned OFF_KM = OFF_VX + 64u * VX_S;
constexpr unsigned KM_S = 16u * 14 * 512;
constexpr unsigned OFF_VM = OFF_KM + 64u * KM_S;
constexpr unsigned VM_S = 16u * 28 * 512;
constexpr unsigned OFF_WD = OFF_VM + 64u * VM_S;
constexpr unsigned OFF_HF = OFF_WD + 28u * 56 * 1024;
constexpr unsigned HF_S = 2u * 112 * 512;
constexpr unsigned OFF_BF = OFF_HF + 64u * HF_S;
constexpr unsigned BF_S = 2u * 28 * 512;
constexpr unsigned OFF_SXX = OFF_BF + 64u * BF_S;
constexpr unsigned SXX_S = 16u * 3 * 1024;
constexpr unsigned OFF_XF = OFF_SXX + 64u * SXX_S;
constexpr unsigned XF_S = 28u * 1024;
constexpr unsigned OFF_G = OFF_XF + 64u * XF_S;
constexpr unsigned OFF_FL = OFF_G + 8192;
constexpr unsigned FS = 128;
constexpr unsigned FL_T1K = OFF_FL;
constexpr unsigned FL_T1V = FL_T1K + 32 * FS;
constexpr unsigned FL_F1 = FL_T1V + 32 * FS;
constexpr unsigned FL_CH = FL_F1 + 16 * FS;
constexpr unsigned FL_T2 = FL_CH + 64 * FS;
constexpr unsigned FL_F2 = FL_T2 + 64 * FS;
constexpr unsigned FL_POL = FL_F2 + 64 * FS;
constexpr unsigned FL_ROT = FL_POL + FS;
constexpr unsigned FL_Q = FL_POL - FS;
constexpr unsigned FL_COUNT = 32 + 32 + 16 + 64 + 64 + 64 + 1;
constexpr unsigned OFF_DBG = OFF_FL + 65536;
constexpr unsigned OFF_MB = 112u << 20;
constexpr unsigned WS_USED = OFF_MB + (8u << 20);
static_assert(OFF_DBG + (2u << 20) <= OFF_MB, "workspace map");
constexpr unsigned K_SM = 0, K_SX = 32768, K_M = 65536, K_REP = 16384, K_PROBE = 98304;

constexpr unsigned SPIN_MAX = 40000u;
constexpr int LDS_BYTES = 144 * 1024;

#define TUNE 0
struct Params {
  const unsigned short* x;
  const unsigned short* mem0;
  const unsigned short* Wq;
  const unsigned short* Wk;
  const unsigned short* Wv;
  const unsigned short* Wo;
  const unsigned short* Wg;
  const unsigned short* Wu;
  const unsigned short* Wd;
  const unsigned short* gw;
  unsigned short* out;
  unsigned char* ws;
};

#define D_SX 5
#define D_SM 0
#define D_MV 7
#define D_MS 28
#define PROBEDBG 0
#define WARM 1
#define D_GAP 5
#define D_BULK 4

__device__ __forceinline__ unsigned pk(float a, float b) {
  bf16x2 v = {(__bf16)a, (__bf16)b};
  return __builtin_bit_cast(unsigned, v);
}
__device__ __forceinline__ unsigned san(unsigned v) { return min(v, 0xFFFFFFFEu); }
__device__ __forceinline__ float bflo(unsigned v) { return __builtin_bit_cast(float, v << 16); }
__device__ __forceinline__ float bfhi(unsigned v) { return __builtin_bit_cast(float, v & 0xFFFF0000u); }
__device__ __forceinline__ float vmax(float a, float b) { return __builtin_elementwise_maximum(a, b); }
template <int N> __device__ __forceinline__ void delay_c() {
#pragma unroll 1
  for (int i = 0; i < N; ++i) __builtin_amdgcn_s_sleep(1);
}
__device__ __forceinline__ void delay_r(int n) {
#pragma unroll 1
  for (int i = 0; i < n; ++i) __builtin_amdgcn_s_sleep(1);
}
#define DELAY(NAME, K) delay_c<NAME>()
__device__ __forceinline__ float max16(float v) {
  unsigned u = __builtin_bit_cast(unsigned, v);
  auto rr = __builtin_amdgcn_permlane16_swap(u, u, false, false);
  const unsigned r0 = rr[0], r1 = rr[1];
  return vmax(__builtin_bit_cast(float, r0), __builtin_bit_cast(float, r1));
}
__device__ __forceinline__ float max32(float v) {
  unsigned u = __builtin_bit_cast(unsigned, v);
  auto rr = __builtin_amdgcn_permlane32_swap(u, u, false, false);
  const unsigned r0 = rr[0], r1 = rr[1];
  return vmax(__builtin_bit_cast(float, r0), __builtin_bit_cast(float, r1));
}
__device__ __forceinline__ float sum16(float v) {
  unsigned u = __builtin_bit_cast(unsigned, v);
  auto rr = __builtin_amdgcn_permlane16_swap(u, u, false, false);
  const unsigned r0 = rr[0], r1 = rr[1];
  return __builtin_bit_cast(float, r0) + __builtin_bit_cast(float, r1);
}
__device__ __forceinline__ float sum32(float v) {
  unsigned u = __builtin_bit_cast(unsigned, v);
  auto rr = __builtin_amdgcn_permlane32_swap(u, u, false, false);
  const unsigned r0 = rr[0], r1 = rr[1];
  return __builtin_bit_cast(float, r0) + __builtin_bit_cast(float, r1);
}
__device__ __forceinline__ bf16x8 as_b8(u32x4 v) { return __builtin_bit_cast(bf16x8, v); }
__device__ __forceinline__ s16x4 as_s4(u32x2 v) { return __builtin_bit_cast(s16x4, v); }
__device__ __forceinline__ bf16x16 cat16(bf16x8 a, bf16x8 b) {
  return __builtin_shufflevector(a, b, 0, 1, 2, 3, 4, 5, 6, 7, 8, 9, 10, 11, 12, 13, 14, 15);
}
__device__ __forceinline__ f32x4 mfma32(bf16x8 a, bf16x8 b, f32x4 c) { return __builtin_amdgcn_mfma_f32_16x16x32_bf16(a, b, c, 0, 0, 0); }
__device__ __forceinline__ f32x4 mfma16(s16x4 a, s16x4 b, f32x4 c) { return __builtin_amdgcn_mfma_f32_16x16x16bf16_1k(a, b, c, 0, 0, 0); }
__device__ __forceinline__ f32x4 smf(bf16x8 a, bf16x16 b, f32x4 c, int idx) { return __builtin_amdgcn_smfmac_f32_16x16x64_bf16(a, b, c, idx, 0, 0); }
__device__ __forceinline__ u32x4 ld16(rsrc_t rs, unsigned v, unsigned s) { return __builtin_amdgcn_raw_buffer_load_b128(rs, v, s, AUX_SC1); }
__device__ __forceinline__ u32x2 ld8(rsrc_t rs, unsigned v, unsigned s) { return __builtin_amdgcn_raw_buffer_load_b64(rs, v, s, AUX_SC1); }
__device__ __forceinline__ unsigned ld4(rsrc_t rs, unsigned v, unsigned s) { return __builtin_amdgcn_raw_buffer_load_b32(rs, v, s, AUX_SC1); }
__device__ __forceinline__ void st16(u32x4 d, rsrc_t rs, unsigned v, unsigned s) { __builtin_amdgcn_raw_buffer_store_b128(d, rs, v, s, AUX_SC1); }
__device__ __forceinline__ void st8(u32x2 d, rsrc_t rs, unsigned v, unsigned s) { __builtin_amdgcn_raw_buffer_store_b64(d, rs, v, s, AUX_SC1); }
__device__ __forceinline__ void st4(unsigned d, rsrc_t rs, unsigned v, unsigned s) { __builtin_amdgcn_raw_buffer_store_b32(d, rs, v, s, AUX_SC1); }
__device__ __forceinline__ rsrc_t ws_rsrc(unsigned char* ws) { return __builtin_amdgcn_make_buffer_rsrc(ws, 0, WS_USED, 0x00020000); }
__device__ __forceinline__ void glds16(const void* src, LAS unsigned char* dst) {
  __builtin_amdgcn_global_load_lds((const GAS unsigned*)src, (LAS unsigned*)dst, 16, 0, 0);
}
__device__ __forceinline__ u32x4 ws_desc(unsigned char* ws) {
  const unsigned long long a = (unsigned long long)ws;
  return u32x4{(unsigned)a, (unsigned)(a >> 32) & 0xFFFFu, WS_USED, 0x00020000u};
}
#define ALD16(dst, voff, desc, soff) asm volatile("buffer_load_dwordx4 %0, %1, %2, %3 offen sc1" : "=&v"(dst) : "v"(voff), "s"(desc), "s"(soff) : "memory")
#define ALD8(dst, voff, desc, soff) asm volatile("buffer_load_dwordx2 %0, %1, %2, %3 offen sc1" : "=&v"(dst) : "v"(voff), "s"(desc), "s"(soff) : "memory")
#define KEEP(x) asm volatile("" ::"v"(x))
#define BAR() do { asm volatile("s_waitcnt lgkmcnt(0)" ::: "memory"); __builtin_amdgcn_s_barrier(); asm volatile("" ::: "memory"); } while (0)
#define VMWAIT0() asm volatile("s_waitcnt vmcnt(0)" ::: "memory")
#define SCHED() __builtin_amdgcn_sched_barrier(0)
#define TIMING 0
#define DBG_DECL
#define DT(slot) do { } while (0)
#define DBG_END(s, last) do { } while (0)
#define DBG_OUT(P, wg, wave) do { } while (0)
#define STAMP(P, slot) do { } while (0)
#define CNT(P, slot, v) do { } while (0)

__device__ __forceinline__ bool any_sent4(u32x4 v) {
  unsigned m = max(max(v.x, v.y), max(v.z, v.w));
  return __builtin_amdgcn_ballot_w64(m == SENT) != 0ull;
}
__device__ __forceinline__ bool any_sent2(u32x2 a, u32x2 b) {
  unsigned m = max(max(a.x, a.y), max(b.x, b.y));
  return __builtin_amdgcn_ballot_w64(m == SENT) != 0ull;
}
__device__ __forceinline__ unsigned mbase(int s, int pol) {
  return OFF_MB + (unsigned)s * 131072u + (((unsigned)pol >> (s >> 4)) & 1u) * 8192u;
}
__device__ __forceinline__ int wait_flags2(rsrc_t rs, unsigned off0, unsigned off1, int need, int& tmo) {
  const int lane = threadIdx.x & 63;
  const unsigned vo = (lane & 1) ? off1 : off0;
  int lo = 0;
  for (unsigned sp = 0;; ++sp) {
    asm volatile("" ::: "memory");
    int v = (int)ld4(rs, vo, 0);
    lo = min(__builtin_amdgcn_readlane(v, 0), __builtin_amdgcn_readlane(v, 1));
    if (lo >= need) break;
    if (sp > SPIN_MAX || tmo) { tmo = 1; lo = NSEG + 1; break; }
    __builtin_amdgcn_s_sleep(4);
  }
  return lo;
}

__global__ void __launch_bounds__(512, 2) prep_kernel(Params P) {
  extern __shared__ __attribute__((aligned(16))) unsigned char smem[];
  LAS unsigned char* lds = (LAS unsigned char*)smem;
  const int tid = threadIdx.x, lane = tid & 63, wave = __builtin_amdgcn_readfirstlane(tid >> 6);
  const int b = blockIdx.x, xcd = b & 7, j = b >> 3;
  const int wh = xcd * 4 + (j >> 3), which = wh >> 4, h = wh & 15, rb = (j >> 2) & 1, cq = j & 3;
  const int n = lane & 15, g = lane >> 4;
  const rsrc_t rs = ws_rsrc(P.ws);
  if (b == 0 && wave == 0) STAMP(P, 8);

  if (which == 0) {
#pragma unroll
    for (int k = 0; k < 7; ++k) {
      const int fr = wave * 7 + k;
      const int ct = 7 * cq + (fr >> 3), ke = fr & 7;
      const int dp = 32 * (ct >> 1) + 8 * (n >> 2) + 4 * (ct & 1) + (n & 3);
      glds16(P.Wq + ((size_t)(dp * 16 + h) * 256 + 32 * ke + 8 * g), lds + fr * 1024);
    }
  } else {
#pragma unroll
    for (int k = 0; k < 7; ++k) {
      const int i = (wave * 7 + k) * 64 + lane;
      const int e = i / 14, ch = i - e * 14;
      glds16(P.Wo + ((size_t)(h * 256 + e) * 448 + 112 * cq + 8 * ch), lds + (wave * 7 + k) * 1024);
    }
  }
  bf16x8 a[2][8];
  const int kk = 7 * rb + (wave < 7 ? wave : 0);
  {
    const unsigned short* W1 = which ? P.Wv : P.Wk;
#pragma unroll
    for (int T = 0; T < 2; ++T) {
      const int d = 32 * kk + 8 * (n >> 2) + 4 * T + (n & 3);
      const unsigned short* p = W1 + ((size_t)(d * 16 + h) * 256 + 8 * g);
#pragma unroll
      for (int ke = 0; ke < 8; ++ke) a[T][ke] = *(const bf16x8*)(p + 32 * ke);
    }
  }
  VMWAIT0();
  __syncthreads();
  if (b == 0 && wave == 0) STAMP(P, 10);
  {
    const u32x4 sv = {SENT, SENT, SENT, SENT};
#pragma unroll
    for (int k = 0; k < 4; ++k) {
      const unsigned o = (unsigned)b * 32768u + (unsigned)(k * 512 + tid) * 16u;
      if (!(o >= K_M && o < K_M + 32768u)) st16(sv, rs, OFF_MB + o, 0);
    }
    if (b == 2) {
      for (int i = tid; i < 1792; i += 512) {
        const int ks = i >> 8, gg = (i >> 6) & 3, hi = (i >> 5) & 1, r = (i >> 2) & 7, p = i & 3;
        const unsigned v = san(*(const unsigned*)(P.mem0 + r * DM + 64 * ks + 16 * gg + 4 * p + 2 * hi));
#pragma unroll
        for (int c = 0; c < 4; ++c) st4(v, rs, OFF_MB + K_M + (unsigned)c * 8192u + (unsigned)i * 4u, 0);
      }
    }
    {
      const int i = b * 512 + tid;
      if (i < 64 * 28 * 64) {
        const int fr = i >> 6, l = i & 63, s = fr / 28, k = fr - s * 28, q = k >= 14 ? 1 : 0, kk = k - 14 * q;
        *(u32x4*)(P.ws + OFF_XF + (size_t)i * 16) = *(const u32x4*)(P.x + (size_t)(32 * s + 16 * q + (l & 15)) * DM + 32 * kk + 8 * (l >> 4));
      }
    }
    if (b == 0 && tid == 0) *(unsigned*)(P.ws + FL_ROT) = (unsigned)__builtin_amdgcn_s_getreg(20 | (3 << 11)) & 7u;
    if (b == 3 && tid < (int)FL_COUNT) st4(tid == (int)FL_COUNT - 1 ? SENT : 0u, rs, OFF_FL + (unsigned)tid * FS, 0);
  }
  if (wave == 7) {
    unsigned short v[7][8];
#pragma unroll
    for (int k = 0; k < 7; ++k) {
      const int fr = min(b + 256 * k, 28 * 56 - 1);
      const int cb = fr / 56, u = fr - cb * 56;
#pragma unroll
      for (int jj = 0; jj < 8; ++jj) v[k][jj] = P.Wd[(size_t)(32 * u + 16 * (jj >> 2) + 4 * g + (jj & 3)) * DM + 16 * cb + n];
    }
#pragma unroll
    for (int k = 0; k < 7; ++k) {
      const int fr = b + 256 * k;
      const u32x4 o = {v[k][0] | ((unsigned)v[k][1] << 16), v[k][2] | ((unsigned)v[k][3] << 16), v[k][4] | ((unsigned)v[k][5] << 16), v[k][6] | ((unsigned)v[k][7] << 16)};
      if (fr < 28 * 56) *(u32x4*)(P.ws + OFF_WD + (size_t)fr * 1024 + lane * 16) = o;
    }
    return;
  }
  f32x4 acc[2][7];
#pragma unroll
  for (int c = 0; c < 7; ++c) { acc[0][c] = f32x4{0, 0, 0, 0}; acc[1][c] = f32x4{0, 0, 0, 0}; }
  if (which == 0) {
#pragma unroll
    for (int ke = 0; ke < 8; ++ke)
#pragma unroll
      for (int c = 0; c < 7; ++c) {
        const bf16x8 bb = *(const LAS bf16x8*)(lds + (c * 8 + ke) * 1024 + lane * 16);
        acc[0][c] = mfma32(a[0][ke], bb, acc[0][c]);
        acc[1][c] = mfma32(a[1][ke], bb, acc[1][c]);
      }
  } else {
    const LAS unsigned char* tb = lds + (8 * g + (n >> 2)) * 224 + (n & 3) * 8;
#pragma unroll
    for (int ke = 0; ke < 8; ++ke)
#pragma unroll
      for (int c = 0; c < 7; ++c) {
        typedef short v4s __attribute__((ext_vector_type(4)));
        const v4s lo = __builtin_amdgcn_ds_read_tr16_b64_v4i16((LAS v4s*)(tb + ke * 32 * 224 + c * 32));
        const v4s hi = __builtin_amdgcn_ds_read_tr16_b64_v4i16((LAS v4s*)(tb + ke * 32 * 224 + 4 * 224 + c * 32));
        const bf16x8 bb = __builtin_bit_cast(bf16x8, __builtin_shufflevector(lo, hi, 0, 1, 2, 3, 4, 5, 6, 7));
        acc[0][c] = mfma32(a[0][ke], bb, acc[0][c]);
        acc[1][c] = mfma32(a[1][ke], bb, acc[1][c]);
      }
  }
  const float sc = which ? 1.0f : 0.0625f * 1.4426950408889634f;
  unsigned char* dst = P.ws + (which ? OFF_WV : OFF_WK) + ((size_t)(h * 28 + 7 * cq) * 14 + kk) * 1024 + lane * 16;
#pragma unroll
  for (int c = 0; c < 7; ++c) {
    const f32x4 u = acc[0][c] * sc, v = acc[1][c] * sc;
    const u32x4 o = {pk(u[0], u[1]), pk(u[2], u[3]), pk(v[0], v[1]), pk(v[2], v[3])};
    *(u32x4*)(dst + (size_t)c * 14 * 1024) = o;
  }
  if (b == 0 && wave == 0) STAMP(P, 9);
}

__device__ __forceinline__ u32x4 gather_m(const Params& P, u32x4 dsc, unsigned voff, unsigned so0, LAS unsigned char* dst, bool act, int& tmo, unsigned& nfail) {
  u32x4 va, vb;
  ALD16(va, voff, dsc, so0);
  DELAY(D_GAP, 4);
  const unsigned so1 = so0 + K_REP;
  ALD16(vb, voff, dsc, so1);
  asm volatile("s_waitcnt vmcnt(1)" : "+v"(va)::"memory");
  if (__builtin_expect(any_sent4(va), 0)) {
    if (TIMING) ++nfail;
    asm volatile("s_waitcnt vmcnt(0)" : "+v"(vb)::"memory");
    va = vb;
    for (unsigned sp = 2; any_sent4(va); ++sp) {
      asm volatile("" ::: "memory");
      const unsigned r = sp % 3u, so = r == 2u ? (so0 ^ 8192u) : so0 + r * K_REP;
      ALD16(va, voff, dsc, so);
      asm volatile("s_waitcnt vmcnt(0)" : "+v"(va)::"memory");
      if (sp > SPIN_MAX || tmo) { tmo = 1; break; }
    }
  }
  if (act) *(LAS u32x4*)dst = va;
  return vb;
}

__device__ __forceinline__ int get_pol(rsrc_t rs, int& tmo) {
  unsigned v;
  for (unsigned sp = 0;; ++sp) {
    asm volatile("" ::: "memory");
    v = (unsigned)__builtin_amdgcn_readfirstlane((int)ld4(rs, 0, FL_POL));
    if (v != SENT) break;
    if (sp > SPIN_MAX) { tmo = 1; v = 0; break; }
    __builtin_amdgcn_s_sleep(2);
  }
  return (int)v;
}

__device__ __forceinline__ void probe_pol(const Params& P) {
  if (threadIdx.x >= 64) return;
  const rsrc_t rs = ws_rsrc(P.ws);
  __builtin_amdgcn_s_sleep(64);
  unsigned pol = 0;
  for (int round = 0; round < 3; ++round) {
    int d[4], dsum = 0;
#pragma unroll
    for (int p = 0; p < 4; ++p) {
      unsigned t[2];
#pragma unroll
      for (int cls = 0; cls < 2; ++cls) {
        const unsigned off = OFF_MB + (unsigned)(p * 16 + round) * 131072u + K_PROBE + (unsigned)cls * 8192u;
        const unsigned long long c0 = __builtin_amdgcn_s_memtime();
        unsigned v = ld4(rs, 0, off);
        asm volatile("s_waitcnt vmcnt(0)" : "+v"(v)::"memory");
        const unsigned long long c1 = __builtin_amdgcn_s_memtime();
        t[cls] = (unsigned)(c1 - c0);
      }
      d[p] = max(-300, min(300, (int)t[0] - (int)t[1]));
      dsum += (p & 1) ? -d[p] : d[p];
    }
    pol = 0;
#pragma unroll
    for (int p = 0; p < 4; ++p) pol |= (((dsum > 0) ? 1u : 0u) ^ (unsigned)(p & 1)) << p;
    if (dsum > 450 || dsum < -450) break;
  }
  if (threadIdx.x == 0) {
    st4(pol, rs, FL_POL, 0);
  }
}

__device__ __forceinline__ void warm_tlb(const Params& P) {
  unsigned z = 0;
  asm volatile("" : "+v"(z));
  unsigned acc = 0;
  constexpr unsigned N1 = (OFF_WD - OFF_KX) >> 16, N2 = 8u << 4;
  for (unsigned i = threadIdx.x; i < N1 + N2 + 1; i += 512) {
    const unsigned off = i < N1 ? OFF_KX + (i << 16) : OFF_MB + ((i - N1) << 16) + K_PROBE + 4096;
    if (i < N1 + N2) acc += __hip_atomic_fetch_add((GAS unsigned*)(P.ws + off), z, __ATOMIC_RELAXED, __HIP_MEMORY_SCOPE_AGENT);
  }
  asm volatile("" ::"v"(acc));
}

__device__ __forceinline__ void score_main(const Params& P, int idx, int wave, LAS unsigned char* lds) {
  const int lane = threadIdx.x & 63;
  const int h = idx >> 1, q = idx & 1;
  const rsrc_t rs = ws_rsrc(P.ws);
  LAS unsigned char* img = lds;
  LAS float* red = (LAS float*)(lds + 8192);
  const int g = lane >> 4, n = lane & 15;
  const int idxv = (n & 8) ? 0xEEEE : 0x4444;
  s16x4 E;
#pragma unroll
  for (int jj = 0; jj < 4; ++jj) E[jj] = (((4 * g + jj) & 7) == n) ? (short)0x3F80 : (short)0;
  int tmo = 0;
  unsigned nfail = 0;
  if (idx == 0 && wave == 0) STAMP(P, 0);
  bf16x16 w0[7], w1[7];
  const int Pp = 7 * q + wave;
  {
    const unsigned char* b0 = P.ws + OFF_WK + ((size_t)(h * 28 + 2 * Pp) * 14) * 1024 + lane * 16;
#pragma unroll
    for (int ks = 0; ks < 7; ++ks) {
      w0[ks] = cat16(*(const bf16x8*)(b0 + (2 * ks) * 1024), *(const bf16x8*)(b0 + (2 * ks + 1) * 1024));
      w1[ks] = cat16(*(const bf16x8*)(b0 + (14 + 2 * ks) * 1024), *(const bf16x8*)(b0 + (15 + 2 * ks) * 1024));
    }
  }
  const int pol = get_pol(rs, tmo);
  const unsigned gloc = (unsigned)(wave * 56 + (lane < 56 ? lane : 55)) * 16u;
  const unsigned gvoff = gloc + K_M;
  LAS unsigned char* gdst = img + gloc;
  const LAS unsigned char* afp = img + lane * 16;
  const LAS unsigned char* mp = img + (Pp >> 1) * 1024 + (2 * (Pp & 1) + (g >> 1)) * 256 + (n & 7) * 16 + (g & 1) * 8;
  LAS float* redw = red + wave * 64 + n * 8 + 4 * g;
  const unsigned kmv = OFF_KM + (unsigned)(h * 14 + Pp) * 512u + (unsigned)(g * 8 + n) * 16u;
  const unsigned smv = K_SM + (unsigned)idx * 256u + lane * 4u;
  const u32x4 dsc = ws_desc(P.ws);
  DBG_DECL
  for (int s = 0; s < NSEG; ++s) {
    const unsigned mb = mbase(s, pol);
    if (s > 0) DELAY(D_MS, 0);
    DT(0);
    const u32x4 pend = gather_m(P, dsc, gvoff, mb, gdst, lane < 56, tmo, nfail);
    DT(1);
    BAR();
    DT(2);
    bf16x8 a[7];
#pragma unroll
    for (int ks = 0; ks < 7; ++ks) a[ks] = *(const LAS bf16x8*)(afp + ks * 1024);
    const u32x2 m0 = *(const LAS u32x2*)mp, m1 = *(const LAS u32x2*)(mp + 128);
    SCHED();
    f32x4 c0 = {0, 0, 0, 0}, c1 = {0, 0, 0, 0};
#pragma unroll
    for (int ks = 0; ks < 7; ++ks) {
      c0 = smf(a[ks], w0[ks], c0, idxv);
      c1 = smf(a[ks], w1[ks], c1, idxv);
    }
    const u32x2 t0 = {pk(c0[0], c0[1]), pk(c0[2], c0[3])}, t1 = {pk(c1[0], c1[1]), pk(c1[2], c1[3])};
    const f32x4 z = {0, 0, 0, 0};
    const f32x4 y0 = mfma16(as_s4(t0), E, z), y1 = mfma16(as_s4(t1), E, z);
    const u32x4 kf = {pk(y0[0], y0[1]), pk(y0[2], y0[3]), pk(y1[0], y1[1]), pk(y1[2], y1[3])};
    const u32x4 mf = {m0.x, m1.x, m0.y, m1.y};
    const f32x4 S = mfma32(as_b8(kf), as_b8(mf), z);
    if (n < 8 && g < 2) *(LAS f32x4*)redw = S;
    VMWAIT0();
    KEEP(pend);
    DT(3);
    BAR();
    DT(4);
    if (wave == 0 && s < NSEG - 1) {
      const float v0 = red[lane], v1 = red[64 + lane], v2 = red[128 + lane], v3 = red[192 + lane], v4 = red[256 + lane], v5 = red[320 + lane], v6 = red[384 + lane];
      const float v = ((v0 + v1) + (v2 + v3)) + ((v4 + v5) + v6);
      const unsigned o = san(__builtin_bit_cast(unsigned, v));
      st4(o, rs, smv, mb);
      st4(o, rs, smv + K_REP, mb);
      st4(o, rs, smv, mb ^ 8192u);
    }
    DT(5);
    DBG_END(s, 5);
    if (idx == 0 && wave == 1) STAMP(P, 64 + s);
    if (s < NSEG - 1) DELAY(D_BULK, 5);
    if (n < 8) st16(kf, rs, kmv, (unsigned)s * KM_S);
  }
  DBG_OUT(P, idx, wave);
  VMWAIT0();
  BAR();
  CNT(P, 0, nfail);
  if (idx == 0 && wave == 0) STAMP(P, 1);
}

__device__ __forceinline__ void score_sx(const Params& P, int idx, LAS unsigned char* lds) {
  const int lane = threadIdx.x & 63;
  const int wave = 7;
  const int h = idx >> 1, q = idx & 1;
  const rsrc_t rs = ws_rsrc(P.ws);
  LAS unsigned char* img = lds;
  const int g = lane >> 4, n = lane & 15;
  const int idxv = (n & 8) ? 0xEEEE : 0x4444;
  s16x4 E;
#pragma unroll
  for (int jj = 0; jj < 4; ++jj) E[jj] = (((4 * g + jj) & 7) == n) ? (short)0x3F80 : (short)0;
  int tmo = 0;
  unsigned nfail = 0;
  const int pol = get_pol(rs, tmo);
  if (idx == 0) STAMP(P, 2);
  const unsigned f0 = FL_T1K + (unsigned)(h * 2) * FS, f1 = f0 + FS;
  int known = wait_flags2(rs, f0, f1, 1, tmo);
  if (idx == 0) STAMP(P, 3);
  int fl = 0;
  u32x4 kx[14];
  const unsigned kxv = OFF_KX + (unsigned)((h * 2 + q) * 14) * 1024u + lane * 16;
#pragma unroll
  for (int k = 0; k < 14; ++k) kx[k] = ld16(rs, kxv + k * 1024, 0);
  const unsigned gloc = (unsigned)(wave * 56 + (lane < 56 ? lane : 55)) * 16u;
  const unsigned gvoff = gloc + K_M;
  LAS unsigned char* gdst = img + gloc;
  const LAS unsigned char* afp = img + lane * 16;
  const unsigned sxv = K_SX + (unsigned)idx * 256u + (unsigned)(g * 8 + (n & 7)) * 8u;
  const unsigned chf = FL_CH + (unsigned)idx * FS;
  __builtin_amdgcn_s_setprio(3);
  const u32x4 dsc = ws_desc(P.ws);
  for (int s = 0; s < NSEG; ++s) {
    const unsigned mb = mbase(s, pol);
    if (s > 0) DELAY(D_MS, 0);
    const u32x4 pend = gather_m(P, dsc, gvoff, mb, gdst, lane < 56, tmo, nfail);
    BAR();
    {
      bf16x8 a[7];
#pragma unroll
      for (int ks = 0; ks < 7; ++ks) a[ks] = *(const LAS bf16x8*)(afp + ks * 1024);
      SCHED();
      f32x4 c0 = {0, 0, 0, 0};
#pragma unroll
      for (int ks = 0; ks < 7; ++ks) c0 = smf(a[ks], cat16(as_b8(kx[2 * ks]), as_b8(kx[2 * ks + 1])), c0, idxv);
      VMWAIT0();
      KEEP(pend);
      const u32x2 t0 = {pk(c0[0], c0[1]), pk(c0[2], c0[3])};
      const f32x4 z = {0, 0, 0, 0};
      const f32x4 y0 = mfma16(as_s4(t0), E, z);
      const u32x2 o = {san(pk(y0[0], y0[1])), san(pk(y0[2], y0[3]))};
      if (n < 8 && s < NSEG - 1) {
        st8(o, rs, sxv, mb);
        st8(o, rs, sxv + K_REP, mb);
        st8(o, rs, sxv, mb ^ 8192u);
      }
      if (lane == 0) st4((unsigned)s, rs, chf, 0);
      SCHED();
    }
    BAR();
    if (s < NSEG - 1) DELAY(D_BULK, 5);
    const int sn = min(s + 1, NSEG - 2);
    known = max(known, min(__builtin_amdgcn_readlane(fl, 0), __builtin_amdgcn_readlane(fl, 1)));
    if (__builtin_expect(known <= sn, 0)) { known = wait_flags2(rs, f0, f1, sn + 1, tmo); CNT(P, 4, 1); }
    fl = (int)ld4(rs, (lane & 1) ? f1 : f0, 0);
#pragma unroll
    for (int k = 0; k < 14; ++k) kx[k] = ld16(rs, kxv + k * 1024, (unsigned)sn * KX_S);
    SCHED();
  }
  VMWAIT0();
  BAR();
  if (lane == 0) st4((unsigned)NSEG, rs, chf, 0);
  CNT(P, 0, nfail);
}

__device__ __forceinline__ void value_wg(const Params& P, int cb, LAS unsigned char* lds) {
  const int tid = threadIdx.x, lane = tid & 63, wave = __builtin_amdgcn_readfirstlane(tid >> 6);
  const rsrc_t rs = ws_rsrc(P.ws);
  LAS unsigned char* img = lds;
  LAS float* red = (LAS float*)(lds + 8192);
  const int g = lane >> 4, n = lane & 15, sel = n >> 3, i = n & 7;
  const int idxv = (n & 8) ? 0xEEEE : 0x4444;
  const int h0 = 2 * wave, hs = h0 + sel;
  int tmo = 0;
  unsigned nfail = 0, nfx = 0, nfm = 0;
  bf16x16 w0[7], w1[7];
  {
    const unsigned char* b0 = P.ws + OFF_WV + ((size_t)(h0 * 28 + cb) * 14) * 1024 + lane * 16;
    const unsigned char* b1 = b0 + (size_t)28 * 14 * 1024;
#pragma unroll
    for (int ks = 0; ks < 7; ++ks) {
      w0[ks] = cat16(*(const bf16x8*)(b0 + (2 * ks) * 1024), *(const bf16x8*)(b0 + (2 * ks + 1) * 1024));
      w1[ks] = cat16(*(const bf16x8*)(b1 + (2 * ks) * 1024), *(const bf16x8*)(b1 + (2 * ks + 1) * 1024));
    }
  }
  const int pol = get_pol(rs, tmo);
  const unsigned fa = FL_T1V + (unsigned)(h0 * 2 + (cb >= 14 ? 1 : 0)) * FS, fb = fa + 2 * FS;
  LAS int* kn = (LAS int*)(lds + 16384);
  if (tid == 0) *kn = 0;
  int known = 0, fl = 0;
  const unsigned vxv = OFF_VX + (unsigned)(h0 * 28 + cb) * 1024u + lane * 16;
  const unsigned gloc = (unsigned)(wave * 56 + (lane < 56 ? lane : 55)) * 16u;
  const unsigned gvoff = gloc + K_M;
  LAS unsigned char* gdst = img + gloc;
  const LAS unsigned char* afp = img + lane * 16;
  const unsigned vmv = OFF_VM + (unsigned)(h0 * 28 + cb) * 512u + lane * 8;
  const unsigned sxv = K_SX + (unsigned)(hs * 2) * 256u + (unsigned)(g * 8 + i) * 8u;
  const unsigned smv = K_SM + (unsigned)(hs * 2) * 256u + (unsigned)(i * 8 + 4 * (g & 1)) * 4u;
  LAS float* redw = red + wave * 128 + i * 16 + 4 * g;
  const LAS float* redr = red + ((lane >> 2) & 7) * 16 + 4 * (lane & 3) + 2 * (lane >> 5);
  const unsigned mv = K_M + (unsigned)cb * 256u + lane * 4u;
  const unsigned chf = FL_CH + (unsigned)(32 + cb) * FS;
  u32x2 vm0 = {0, 0}, vm1 = {0, 0};
  const u32x4 dsc = ws_desc(P.ws);
  DBG_DECL
  for (int s = 0; s < NSEG - 1; ++s) {
    const unsigned mb = mbase(s, pol), mbn = mbase(s + 1, pol);
    known = max(known, __builtin_amdgcn_readfirstlane(*(volatile LAS int*)kn));
    if (s > 0) DELAY(D_MV, 1);
    DT(0);
    const u32x4 pend = gather_m(P, dsc, gvoff, mb, gdst, lane < 56, tmo, nfail);
    DT(1);
    BAR();
    if (__builtin_expect(known <= s, 0)) { known = wait_flags2(rs, fa, fb, s + 1, tmo); CNT(P, 5, 1); }
    bf16x8 a[7];
#pragma unroll
    for (int ks = 0; ks < 7; ++ks) a[ks] = *(const LAS bf16x8*)(afp + ks * 1024);
    u32x4 vx0 = ld16(rs, vxv, (unsigned)s * VX_S);
    u32x4 vx1 = ld16(rs, vxv + 28 * 1024, (unsigned)s * VX_S);
    st8(vm0, rs, vmv, (unsigned)max(s - 1, 0) * VM_S);
    st8(vm1, rs, vmv + 28 * 512, (unsigned)max(s - 1, 0) * VM_S);
    if (wave == 1 && lane == 0) st4((unsigned)max(s - 1, 0), rs, chf, 0);
    if (wave == 7) fl = (int)ld4(rs, (unsigned)(lane & 31) * FS, FL_T1V);
    SCHED();
    f32x4 c0 = {0, 0, 0, 0}, c1 = {0, 0, 0, 0};
#pragma unroll
    for (int ks = 0; ks < 7; ++ks) {
      c0 = smf(a[ks], w0[ks], c0, idxv);
      c1 = smf(a[ks], w1[ks], c1, idxv);
    }
    vm0 = u32x2{pk(c0[0], c0[1]), pk(c0[2], c0[3])};
    vm1 = u32x2{pk(c1[0], c1[1]), pk(c1[2], c1[3])};
    SCHED();
    DT(2);
    DELAY(D_SX, 2);
    u32x2 x0, x1, y0, y1;
    {
      const unsigned sxv1 = sxv + 256, mb1 = mb + K_REP;
      ALD8(x0, sxv, dsc, mb);
      ALD8(x1, sxv1, dsc, mb);
      DELAY(D_GAP, 4);
      ALD8(y0, sxv, dsc, mb1);
      ALD8(y1, sxv1, dsc, mb1);
      asm volatile("s_waitcnt vmcnt(2)" : "+v"(x0), "+v"(x1)::"memory");
      KEEP(pend);
      asm volatile("" : "+v"(vx0), "+v"(vx1));
      if (__builtin_expect(any_sent2(x0, x1), 0)) {
        if (TIMING) ++nfx;
        asm volatile("s_waitcnt vmcnt(0)" : "+v"(y0), "+v"(y1)::"memory");
        x0 = y0; x1 = y1;
        for (unsigned sp = 2; any_sent2(x0, x1); ++sp) {
          asm volatile("" ::: "memory");
          const unsigned r = sp % 3u, so = r == 2u ? (mb ^ 8192u) : mb + r * K_REP;
          ALD8(x0, sxv, dsc, so);
          ALD8(x1, sxv1, dsc, so);
          asm volatile("s_waitcnt vmcnt(0)" : "+v"(x0), "+v"(x1)::"memory");
          if (sp > SPIN_MAX || tmo) { tmo = 1; break; }
        }
      }
    }
    DT(3);
    DELAY(D_SM, 3);
    u32x4 ua, ub, wa, wb;
    const unsigned smv1 = smv + 256, mb1 = mb + K_REP;
    ALD16(ua, smv, dsc, mb);
    ALD16(ub, smv1, dsc, mb);
    SCHED();
    float t[8] = {bflo(x0.x), bfhi(x0.x), bflo(x0.y), bfhi(x0.y), bflo(x1.x), bfhi(x1.x), bflo(x1.y), bfhi(x1.y)};
    float m1 = vmax(vmax(vmax(t[0], t[1]), vmax(t[2], t[3])), vmax(vmax(t[4], t[5]), vmax(t[6], t[7])));
    m1 = max32(max16(m1));
    float l1 = 0.f;
#pragma unroll
    for (int k = 0; k < 8; ++k) { t[k] = __builtin_amdgcn_exp2f(t[k] - m1); l1 += t[k]; }
    SCHED();
    ALD16(wa, smv, dsc, mb1);
    ALD16(wb, smv1, dsc, mb1);
    SCHED();
    l1 = sum32(sum16(l1));
    const u32x4 pb = {pk(t[0], t[1]), pk(t[2], t[3]), pk(t[4], t[5]), pk(t[6], t[7])};
    const f32x4 z = {0, 0, 0, 0};
    const f32x4 ax0 = mfma32(as_b8(vx0), as_b8(pb), z), ax1 = mfma32(as_b8(vx1), as_b8(pb), z);
    const f32x4 ax = sel ? ax1 : ax0;
    SCHED();
    DT(4);
    asm volatile("s_waitcnt vmcnt(2)" : "+v"(ua), "+v"(ub)::"memory");
    KEEP(y0); KEEP(y1);
    if (__builtin_expect(any_sent4(ua) || any_sent4(ub), 0)) {
      if (TIMING) ++nfm;
      asm volatile("s_waitcnt vmcnt(0)" : "+v"(wa), "+v"(wb)::"memory");
      ua = wa; ub = wb;
      for (unsigned sp = 2; any_sent4(ua) || any_sent4(ub); ++sp) {
        asm volatile("" ::: "memory");
        const unsigned r = sp % 3u, so = r == 2u ? (mb ^ 8192u) : mb + r * K_REP;
        ALD16(ua, smv, dsc, so);
        ALD16(ub, smv1, dsc, so);
        asm volatile("s_waitcnt vmcnt(0)" : "+v"(ua), "+v"(ub)::"memory");
        if (sp > SPIN_MAX || tmo) { tmo = 1; break; }
      }
    }
    DT(5);
    const f32x4 sm = __builtin_bit_cast(f32x4, ua) + __builtin_bit_cast(f32x4, ub);
    float m2 = vmax(vmax(sm[0], sm[1]), vmax(sm[2], sm[3]));
    m2 = max16(m2);
    const float M = vmax(m1, m2);
    const float al = __builtin_amdgcn_exp2f(m1 - M);
    const float e0 = __builtin_amdgcn_exp2f(sm[0] - M), e1 = __builtin_amdgcn_exp2f(sm[1] - M), e2 = __builtin_amdgcn_exp2f(sm[2] - M), e3 = __builtin_amdgcn_exp2f(sm[3] - M);
    const u32x2 pm = {pk(e0, e1), pk(e2, e3)};
    const f32x4 cx = ax * al;
    const f32x4 r0 = mfma16(as_s4(vm0), as_s4(pm), cx), r1 = mfma16(as_s4(vm1), as_s4(pm), cx);
    const float l2 = sum16((e0 + e1) + (e2 + e3));
    const float rl = __builtin_amdgcn_rcpf(al * l1 + l2);
    f32x4 r = (sel ? r1 : r0) * rl;
#pragma unroll
    for (int k = 0; k < 4; ++k) {
      const float rk = r[k];
      const int o = __builtin_amdgcn_update_dpp(0, __builtin_bit_cast(int, rk), 0x128, 0xf, 0xf, true);
      r[k] += __builtin_bit_cast(float, o);
    }
    if (sel == 0) *(LAS f32x4*)redw = r;
    BAR();
    DT(6);
    if (wave == 0) {
      float a0[8], a1[8];
#pragma unroll
      for (int w = 0; w < 8; ++w) { a0[w] = redr[w * 128]; a1[w] = redr[w * 128 + 1]; }
      const float s0 = ((a0[0] + a0[1]) + (a0[2] + a0[3])) + ((a0[4] + a0[5]) + (a0[6] + a0[7]));
      const float s1 = ((a1[0] + a1[1]) + (a1[2] + a1[3])) + ((a1[4] + a1[5]) + (a1[6] + a1[7]));
      const unsigned o = san(pk(s0, s1));
      st4(o, rs, mv, mbn);
      st4(o, rs, mv + K_REP, mbn);
      st4(o, rs, mv, mbn ^ 8192u);
      asm volatile("s_waitcnt vmcnt(3)" ::: "memory");
    } else {
      VMWAIT0();
      if (wave == 7) {
        int mn = fl;
#pragma unroll
        for (int o = 1; o < 32; o <<= 1) mn = min(mn, __shfl_xor(mn, o));
        if (lane == 0) *kn = mn;
      }
    }
    KEEP(wa); KEEP(wb);
    DT(7);
    DBG_END(s, 7);
  }
  DBG_OUT(P, 32 + cb, wave);
  {
    const int s = NSEG - 1;
    const unsigned mb = mbase(s, pol);
    DELAY(D_MV, 1);
    const u32x4 pend = gather_m(P, dsc, gvoff, mb, gdst, lane < 56, tmo, nfail);
    BAR();
    st8(vm0, rs, vmv, (unsigned)(s - 1) * VM_S);
    st8(vm1, rs, vmv + 28 * 512, (unsigned)(s - 1) * VM_S);
    f32x4 c0 = {0, 0, 0, 0}, c1 = {0, 0, 0, 0};
#pragma unroll
    for (int ks = 0; ks < 7; ++ks) {
      const bf16x8 a = *(const LAS bf16x8*)(afp + ks * 1024);
      c0 = smf(a, w0[ks], c0, idxv);
      c1 = smf(a, w1[ks], c1, idxv);
    }
    vm0 = u32x2{pk(c0[0], c0[1]), pk(c0[2], c0[3])};
    vm1 = u32x2{pk(c1[0], c1[1]), pk(c1[2], c1[3])};
    st8(vm0, rs, vmv, (unsigned)s * VM_S);
    st8(vm1, rs, vmv + 28 * 512, (unsigned)s * VM_S);
    VMWAIT0();
    KEEP(pend);
    BAR();
    if (wave == 1 && lane == 0) st4((unsigned)NSEG, rs, chf, 0);
    CNT(P, 1, nfail); CNT(P, 2, nfx); CNT(P, 3, nfm);
    if (cb == 0 && wave == 0) STAMP(P, 4);
  }
}

__device__ __forceinline__ void stage_x(const Params& P, int s, LAS unsigned char* buf, int lane, int first, int count) {
  const unsigned char* p = P.ws + OFF_XF + (size_t)s * XF_S + lane * 16;
  for (int k = first; k < first + count; ++k) glds16(p + k * 1024, buf + k * 1024);
}

template <int MODE>
__device__ __forceinline__ void stream_wg(const Params& P, int id, LAS unsigned char* lds) {
  const int tid = threadIdx.x, lane = tid & 63, wave = __builtin_amdgcn_readfirstlane(tid >> 6);
  const rsrc_t rs = ws_rsrc(P.ws);
  const int n = lane & 15, g = lane >> 4;
  bf16x8 w0[14], w1[14];
  if (MODE == 2) {
#pragma unroll 1
    for (int m = 0; m < 2; ++m) {
      const unsigned short* W = m ? P.Wu : P.Wg;
      for (int k = wave; k < 98; k += 8) {
        const int i = k * 64 + lane, d = i / 14, ch = i - d * 14;
        glds16(W + (size_t)d * 1792 + 112 * id + 8 * ch, lds + k * 1024);
      }
      VMWAIT0();
      __syncthreads();
      if (wave < 7) {
        const LAS unsigned char* tb = lds + (8 * g + (n >> 2)) * 224 + (n & 3) * 8 + wave * 32;
#pragma unroll
        for (int kk = 0; kk < 14; ++kk) {
          typedef short v4s __attribute__((ext_vector_type(4)));
          const v4s lo = __builtin_amdgcn_ds_read_tr16_b64_v4i16((LAS v4s*)(tb + kk * 32 * 224));
          const v4s hi = __builtin_amdgcn_ds_read_tr16_b64_v4i16((LAS v4s*)(tb + kk * 32 * 224 + 4 * 224));
          const bf16x8 f = __builtin_bit_cast(bf16x8, __builtin_shufflevector(lo, hi, 0, 1, 2, 3, 4, 5, 6, 7));
          if (m == 0) w0[kk] = f; else w1[kk] = f;
        }
      }
      __syncthreads();
    }
  } else if (wave < 7) {
    const int h = id >> 1, hq = id & 1;
    const unsigned char* b0 = P.ws + (MODE ? OFF_WV : OFF_WK) + ((size_t)(h * 28 + 14 * hq + 2 * wave) * 14) * 1024 + lane * 16;
#pragma unroll
    for (int kk = 0; kk < 14; ++kk) { w0[kk] = *(const bf16x8*)(b0 + kk * 1024); w1[kk] = *(const bf16x8*)(b0 + (14 + kk) * 1024); }
  }
  const unsigned fl = (MODE == 0 ? FL_T1K : MODE == 1 ? FL_T1V : FL_F1) + (unsigned)id * FS;
  if (wave == 7) {
    stage_x(P, 0, lds, lane, 0, 28);
    stage_x(P, 1, lds + 28672, lane, 0, 28);
    asm volatile("s_waitcnt vmcnt(28)" ::: "memory");
    int nb = 2;
    for (int s = 0; s < NSEG; ++s) {
      BAR();
      if (id == 0) STAMP(P, 192 + MODE * 64 + s);
      if (s + 2 < NSEG) { stage_x(P, s + 2, lds + nb * 28672, lane, 0, 28); asm volatile("s_waitcnt vmcnt(28)" ::: "memory"); }
      else VMWAIT0();
      nb = nb == 2 ? 0 : nb + 1;
    }
    BAR();
    if (lane == 0) st4((unsigned)NSEG, rs, fl, 0);
  } else {
    int cbuf = 0;
    for (int s = 0; s < NSEG; ++s) {
      BAR();
      if (wave == 0 && s > 0 && lane == 0) st4((unsigned)(s <= 2 ? s : s - 1), rs, fl, 0);
      const LAS unsigned char* xb = lds + cbuf * 28672 + lane * 16;
      cbuf = cbuf == 2 ? 0 : cbuf + 1;
      f32x4 c00 = {0, 0, 0, 0}, c01 = {0, 0, 0, 0}, c10 = {0, 0, 0, 0}, c11 = {0, 0, 0, 0};
      bf16x8 xa[3], xc[3];
      xa[0] = *(const LAS bf16x8*)(xb); xc[0] = *(const LAS bf16x8*)(xb + 14 * 1024);
      xa[1] = *(const LAS bf16x8*)(xb + 1024); xc[1] = *(const LAS bf16x8*)(xb + 15 * 1024);
#pragma unroll
      for (int kk = 0; kk < 14; ++kk) {
        if (kk + 2 < 14) { xa[(kk + 2) % 3] = *(const LAS bf16x8*)(xb + (kk + 2) * 1024); xc[(kk + 2) % 3] = *(const LAS bf16x8*)(xb + (16 + kk) * 1024); }
        SCHED();
        const bf16x8 x0 = xa[kk % 3], x1 = xc[kk % 3];
        if (MODE == 1) { c00 = mfma32(x0, w0[kk], c00); c01 = mfma32(x0, w1[kk], c01); c10 = mfma32(x1, w0[kk], c10); c11 = mfma32(x1, w1[kk], c11); }
        else { c00 = mfma32(w0[kk], x0, c00); c01 = mfma32(w1[kk], x0, c01); c10 = mfma32(w0[kk], x1, c10); c11 = mfma32(w1[kk], x1, c11); }
        SCHED();
      }
      if (MODE == 2) {
#pragma unroll
        for (int q = 0; q < 2; ++q) {
          const f32x4 cg = q ? c10 : c00, cu = q ? c11 : c01;
          float hv[4];
#pragma unroll
          for (int k = 0; k < 4; ++k) {
            const float gg = cg[k];
            hv[k] = gg * __builtin_amdgcn_rcpf(1.0f + __builtin_amdgcn_exp2f(-1.4426950408889634f * gg)) * cu[k];
          }
          const u32x2 o = {pk(hv[0], hv[1]), pk(hv[2], hv[3])};
          st8(o, rs, OFF_HF + (unsigned)(q * 112 + 7 * id + wave) * 512u + lane * 8, (unsigned)s * HF_S);
        }
      } else if (MODE == 0) {
        const int h = id >> 1, Pp = 7 * (id & 1) + wave;
        const u32x4 o0 = {pk(c00[0], c00[1]), pk(c00[2], c00[3]), pk(c01[0], c01[1]), pk(c01[2], c01[3])};
        const u32x4 o1 = {pk(c10[0], c10[1]), pk(c10[2], c10[3]), pk(c11[0], c11[1]), pk(c11[2], c11[3])};
        st16(o0, rs, OFF_KX + (unsigned)((h * 2 + 0) * 14 + Pp) * 1024u + lane * 16, (unsigned)s * KX_S);
        st16(o1, rs, OFF_KX + (unsigned)((h * 2 + 1) * 14 + Pp) * 1024u + lane * 16, (unsigned)s * KX_S);
      } else {
        const int h = id >> 1, cb = 14 * (id & 1) + 2 * wave;
        const u32x4 o0 = {pk(c00[0], c00[1]), pk(c00[2], c00[3]), pk(c10[0], c10[1]), pk(c10[2], c10[3])};
        const u32x4 o1 = {pk(c01[0], c01[1]), pk(c01[2], c01[3]), pk(c11[0], c11[1]), pk(c11[2], c11[3])};
        st16(o0, rs, OFF_VX + (unsigned)(h * 28 + cb) * 1024u + lane * 16, (unsigned)s * VX_S);
        st16(o1, rs, OFF_VX + (unsigned)(h * 28 + cb + 1) * 1024u + lane * 16, (unsigned)s * VX_S);
      }
      if (s < 2) VMWAIT0(); else asm volatile("s_waitcnt vmcnt(2)" ::: "memory");
    }
    VMWAIT0();
    BAR();
  }
}

template <int SLEEP>
__device__ __forceinline__ void wait_lanes(rsrc_t rs, unsigned voff, int need, int& tmo) {
  for (unsigned sp = 0;; ++sp) {
    asm volatile("" ::: "memory");
    const int v = (int)ld4(rs, voff, 0);
    if (__builtin_amdgcn_ballot_w64(v < need) == 0ull) break;
    if (sp > SPIN_MAX || tmo) { tmo = 1; break; }
    __builtin_amdgcn_s_sleep(SLEEP);
  }
}

__device__ __forceinline__ void task_sxx(const Params& P, int s, LAS unsigned char* lds, int& tmo) {
  int tid = threadIdx.x;
  asm volatile("" : "+v"(tid));
  const int lane = tid & 63, wave = __builtin_amdgcn_readfirstlane(tid >> 6);
  const rsrc_t rs = ws_rsrc(P.ws);
  __syncthreads();
  stage_x(P, s, lds, lane, wave * 4, wave < 7 ? 4 : 0);
  if (wave == 0) wait_lanes<24>(rs, FL_T1K + (unsigned)(lane & 31) * FS, s + 1, tmo);
  BAR();
  const unsigned kv = lane * 16;
  const unsigned so = OFF_KX + (unsigned)(2 * wave * 2 * 14) * 1024u + (unsigned)s * KX_S;
  u32x4 ka[2][7], kb[2][7];
#pragma unroll
  for (int k = 0; k < 7; ++k) { ka[0][k] = ld16(rs, kv, so + k * 1024); kb[0][k] = ld16(rs, kv, so + (14 + k) * 1024); }
  asm volatile("s_waitcnt vmcnt(14)" ::: "memory");
  __syncthreads();
  const LAS unsigned char* xb = lds + lane * 16;
  f32x4 c00 = {0, 0, 0, 0}, c01 = {0, 0, 0, 0}, c11 = {0, 0, 0, 0};
#pragma unroll
  for (int c = 0; c < 4; ++c) {
    if (c + 1 < 4) {
      const int hn = (c + 1) >> 1, kn = ((c + 1) & 1) * 7;
#pragma unroll
      for (int k = 0; k < 7; ++k) { ka[(c + 1) & 1][k] = ld16(rs, kv, so + (hn * 28 + kn + k) * 1024); kb[(c + 1) & 1][k] = ld16(rs, kv, so + (hn * 28 + 14 + kn + k) * 1024); }
    }
    SCHED();
#pragma unroll
    for (int k = 0; k < 7; ++k) {
      const int kk = (c & 1) * 7 + k;
      const bf16x8 x0 = *(const LAS bf16x8*)(xb + kk * 1024), x1 = *(const LAS bf16x8*)(xb + (14 + kk) * 1024);
      c00 = mfma32(as_b8(ka[c & 1][k]), x0, c00);
      c01 = mfma32(as_b8(ka[c & 1][k]), x1, c01);
      c11 = mfma32(as_b8(kb[c & 1][k]), x1, c11);
    }
    if (c & 1) {
      const unsigned o = OFF_SXX + (unsigned)((2 * wave + (c >> 1)) * 3) * 1024u + (unsigned)s * SXX_S;
      st16(__builtin_bit_cast(u32x4, c00), rs, kv, o);
      st16(__builtin_bit_cast(u32x4, c01), rs, kv, o + 1024);
      st16(__builtin_bit_cast(u32x4, c11), rs, kv, o + 2048);
      c00 = f32x4{0, 0, 0, 0}; c01 = f32x4{0, 0, 0, 0}; c11 = f32x4{0, 0, 0, 0};
    }
    SCHED();
  }
  VMWAIT0();
  __syncthreads();
  if (tid == 0) st4(1u, rs, FL_T2 + (unsigned)s * FS, 0);
}

__device__ __forceinline__ void task_ffn2(const Params& P, int sp, LAS unsigned char* lds, int& tmo) {
  int tid = threadIdx.x;
  asm volatile("" : "+v"(tid));
  const int lane = tid & 63, wave = __builtin_amdgcn_readfirstlane(tid >> 6);
  const rsrc_t rs = ws_rsrc(P.ws);
  __syncthreads();
  const unsigned char* wd = P.ws + OFF_WD + (size_t)(4 * (wave < 7 ? wave : 0)) * 56 * 1024 + lane * 16;
  bf16x8 wf[4][4];
#pragma unroll
  for (int uu = 0; uu < 4; ++uu)
#pragma unroll
    for (int c = 0; c < 4; ++c) wf[uu][c] = *(const bf16x8*)(wd + (size_t)(c * 56 + uu) * 1024);
  if (wave == 0) wait_lanes<24>(rs, FL_F1 + (unsigned)(lane & 15) * FS, 2 * sp + 2, tmo);
  BAR();
  f32x4 acc[4][4];
#pragma unroll
  for (int c = 0; c < 4; ++c)
#pragma unroll
    for (int q = 0; q < 4; ++q) acc[c][q] = f32x4{0, 0, 0, 0};
#pragma unroll 1
  for (int half = 0; half < 2; ++half) {
    if (half) __syncthreads();
    {
      u32x4 v[14];
#pragma unroll
      for (int k = 0; k < 14; ++k) {
        const int pc = wave * 14 + k, tile = pc / 28, f2 = pc - tile * 28;
        v[k] = ld16(rs, lane * 16, OFF_HF + (unsigned)(2 * sp + (tile >> 1)) * HF_S + (unsigned)((tile & 1) * 112 + 56 * half + 2 * f2) * 512u);
      }
#pragma unroll
      for (int k = 0; k < 14; ++k) *(LAS u32x4*)(lds + (wave * 14 + k) * 1024 + lane * 16) = v[k];
    }
    __syncthreads();
    if (wave < 7) {
      const LAS unsigned char* hb = lds + lane * 8;
#pragma unroll 1
      for (int u0 = 0; u0 < 28; u0 += 4) {
        const int ug = 28 * half + u0;
        const int un = ug + 4 < 56 ? ug + 4 : ug;
#pragma unroll
        for (int uu = 0; uu < 4; ++uu) {
          const int u = u0 + uu;
          u32x4 hf[4];
#pragma unroll
          for (int q = 0; q < 4; ++q) {
            const u32x2 lo = *(const LAS u32x2*)(hb + (q * 56 + 2 * u) * 512), hi = *(const LAS u32x2*)(hb + (q * 56 + 2 * u + 1) * 512);
            hf[q] = u32x4{lo.x, lo.y, hi.x, hi.y};
          }
#pragma unroll
          for (int q = 0; q < 4; ++q)
#pragma unroll
            for (int c = 0; c < 4; ++c) acc[c][q] = mfma32(wf[uu][c], as_b8(hf[q]), acc[c][q]);
          SCHED();
#pragma unroll
          for (int c = 0; c < 4; ++c) wf[uu][c] = *(const bf16x8*)(wd + (size_t)(c * 56 + un + uu) * 1024);
          SCHED();
        }
      }
    } else if (half == 0) {
      const unsigned short* xp = P.x + (size_t)(64 * sp + lane) * DM;
      float acc1 = 0.f;
#pragma unroll 7
      for (int k = 0; k < 56; ++k) {
        const u32x4 xv = *(const u32x4*)(xp + 8 * k), gv = *(const u32x4*)(P.gw + 8 * k);
        acc1 += bflo(xv.x) * bflo(gv.x) + bfhi(xv.x) * bfhi(gv.x) + bflo(xv.y) * bflo(gv.y) + bfhi(xv.y) * bfhi(gv.y) +
                bflo(xv.z) * bflo(gv.z) + bfhi(xv.z) * bfhi(gv.z) + bflo(xv.w) * bflo(gv.w) + bfhi(xv.w) * bfhi(gv.w);
      }
      const float gate = __builtin_amdgcn_rcpf(1.0f + __builtin_amdgcn_exp2f(-1.4426950408889634f * acc1));
      st4(__builtin_bit_cast(unsigned, gate), rs, (unsigned)lane * 4u, OFF_G + (unsigned)(64 * sp) * 4u);
    }
  }
  if (wave < 7) {
#pragma unroll
    for (int c = 0; c < 4; ++c)
#pragma unroll
      for (int q = 0; q < 4; ++q) {
        const u32x2 o = {pk(acc[c][q][0], acc[c][q][1]), pk(acc[c][q][2], acc[c][q][3])};
        st8(o, rs, lane * 8, OFF_BF + (unsigned)((q & 1) * 28 + 4 * wave + c) * 512u + (unsigned)(2 * sp + (q >> 1)) * BF_S);
      }
  }
  VMWAIT0();
  __syncthreads();
  if (tid == 0) st4(1u, rs, FL_F2 + (unsigned)sp * FS, 0);
}

__device__ __forceinline__ void task_attn(const Params& P, int s, int cbg, LAS unsigned char* lds, int& tmo) {
  int tid = threadIdx.x;
  asm volatile("" : "+v"(tid));
  const int lane = tid & 63, wave = __builtin_amdgcn_readfirstlane(tid >> 6);
  const rsrc_t rs = ws_rsrc(P.ws);
  const int n = lane & 15, g = lane >> 4;
  LAS unsigned char* PT = lds + 28672;
  LAS unsigned char* PM = PT + 32768;
  LAS unsigned char* VS = PM + 16384;
  __syncthreads();
  stage_x(P, s, lds, lane, wave * 4, wave < 7 ? 4 : 0);
  {
    const unsigned fo = lane < 32 ? FL_T1V + (unsigned)lane * FS : (lane & 1) ? FL_T2 + (unsigned)s * FS : FL_F2 + (unsigned)(s >> 1) * FS;
    if (wave == 0) wait_lanes<24>(rs, fo, lane < 32 ? s + 1 : 1, tmo);
    BAR();
  }
  const int cb = 4 * cbg + (wave >> 1), qo = wave & 1;
  f32x4 sxx[2][3];
  u32x2 bf;
  float gate;
  {
    u32x4 vx[8];
#pragma unroll
    for (int k = 0; k < 8; ++k) vx[k] = ld16(rs, lane * 16, OFF_VX + (unsigned)((2 * wave + (k >> 2)) * 28 + 4 * cbg + (k & 3)) * 1024u + (unsigned)s * VX_S);
#pragma unroll
    for (int hh = 0; hh < 2; ++hh)
#pragma unroll
      for (int k = 0; k < 3; ++k) sxx[hh][k] = __builtin_bit_cast(f32x4, ld16(rs, lane * 16, OFF_SXX + (unsigned)((2 * wave + hh) * 3 + k) * 1024u + (unsigned)s * SXX_S));
    bf = ld8(rs, lane * 8, OFF_BF + (unsigned)(qo * 28 + cb) * 512u + (unsigned)s * BF_S);
    gate = __builtin_bit_cast(float, ld4(rs, (unsigned)n * 4u, OFF_G + (unsigned)(32 * s + 16 * qo) * 4u));
#pragma unroll
    for (int k = 0; k < 8; ++k) *(LAS u32x4*)(VS + ((2 * wave + (k >> 2)) * 4 + (k & 3)) * 1024 + lane * 16) = vx[k];
  }
  VMWAIT0();
  __syncthreads();
  if (s == NSEG - 1 && cbg == 0 && wave == 0) STAMP(P, 15);
  if (wave == 0) {
    const unsigned vo = FL_CH + (unsigned)(lane < 60 ? lane : 0) * FS;
    for (unsigned sp = 0;; ++sp) {
      asm volatile("" ::: "memory");
      const int v = (int)ld4(rs, vo, 0);
      if (__builtin_amdgcn_ballot_w64(v < s + 1) == 0ull) break;
      if (sp > SPIN_MAX || tmo) { tmo = 1; break; }
      const int v0 = __builtin_amdgcn_readfirstlane(v);
      const int far = min(s - v0 - 1, 16);
      for (int k = 0; k < far; ++k) __builtin_amdgcn_s_sleep(48);
      __builtin_amdgcn_s_sleep(8);
    }
  }
  BAR();
  if (s == NSEG - 1 && cbg == 0 && wave == 0) STAMP(P, 16);
  u32x4 km[14];
#pragma unroll
  for (int kk = 0; kk < 14; ++kk) km[kk] = ld16(rs, (unsigned)((n >> 3) * 14 * 512 + (g * 8 + (n & 7)) * 16), OFF_KM + (unsigned)(2 * wave * 14 + kk) * 512u + (unsigned)s * KM_S);
  u32x2 vm[16];
#pragma unroll
  for (int h = 0; h < 16; ++h) vm[h] = ld8(rs, lane * 8, OFF_VM + (unsigned)(h * 28 + cb) * 512u + (unsigned)s * VM_S);
  SCHED();
  const LAS unsigned char* xb = lds + lane * 16;
  f32x4 s0 = {0, 0, 0, 0}, s1 = {0, 0, 0, 0};
#pragma unroll
  for (int kk = 0; kk < 14; ++kk) {
    s0 = mfma32(as_b8(km[kk]), *(const LAS bf16x8*)(xb + kk * 1024), s0);
    s1 = mfma32(as_b8(km[kk]), *(const LAS bf16x8*)(xb + (14 + kk) * 1024), s1);
  }
  if (s == NSEG - 1 && cbg == 0 && wave == 0) STAMP(P, 17);
  f32x4 smq[2][2];
#pragma unroll
  for (int q = 0; q < 2; ++q)
#pragma unroll
    for (int k = 0; k < 4; ++k) {
      const float v = q ? s1[k] : s0[k];
      const unsigned u = __builtin_bit_cast(unsigned, v);
      auto rr = __builtin_amdgcn_permlane32_swap(u, u, false, false);
      const unsigned r0 = rr[0], r1 = rr[1];
      smq[0][q][k] = __builtin_bit_cast(float, r0);
      smq[1][q][k] = __builtin_bit_cast(float, r1);
    }
#pragma unroll
  for (int hh = 0; hh < 2; ++hh) {
    const int h = 2 * wave + hh;
#pragma unroll
    for (int q = 0; q < 2; ++q) {
      const f32x4 sm = smq[hh][q];
      const f32x4 ta = q ? sxx[hh][1] : sxx[hh][0];
      const f32x4 tb = sxx[hh][2];
      float t[8];
      const float NEG = -3.0e38f;
#pragma unroll
      for (int k = 0; k < 4; ++k) {
        const bool causal = (4 * g + k) <= n;
        t[k] = q ? ta[k] : (causal ? ta[k] : NEG);
        t[4 + k] = q ? (causal ? tb[k] : NEG) : NEG;
      }
      float m = vmax(vmax(vmax(sm[0], sm[1]), vmax(sm[2], sm[3])), vmax(vmax(vmax(t[0], t[1]), vmax(t[2], t[3])), vmax(vmax(t[4], t[5]), vmax(t[6], t[7]))));
      m = max32(max16(m));
      float e[4], l = 0.f;
#pragma unroll
      for (int k = 0; k < 4; ++k) { e[k] = __builtin_amdgcn_exp2f(sm[k] - m); l += e[k]; }
      l *= 0.5f;
#pragma unroll
      for (int k = 0; k < 8; ++k) { t[k] = __builtin_amdgcn_exp2f(t[k] - m); l += t[k]; }
      l = sum32(sum16(l));
      const float rl = __builtin_amdgcn_rcpf(l);
      const u32x4 pt = {pk(t[0] * rl, t[1] * rl), pk(t[2] * rl, t[3] * rl), pk(t[4] * rl, t[5] * rl), pk(t[6] * rl, t[7] * rl)};
      const u32x2 pm = {pk(e[0] * rl, e[1] * rl), pk(e[2] * rl, e[3] * rl)};
      *(LAS u32x4*)(PT + (h * 2 + q) * 1024 + lane * 16) = pt;
      *(LAS u32x2*)(PM + (h * 2 + q) * 512 + lane * 8) = pm;
    }
  }
  __syncthreads();
  if (s == NSEG - 1 && cbg == 0 && wave == 0) STAMP(P, 18);
  f32x4 acc = {0, 0, 0, 0}, accm = {0, 0, 0, 0};
#pragma unroll
  for (int h = 0; h < 16; ++h) {
    const bf16x8 pt = *(const LAS bf16x8*)(PT + (h * 2 + qo) * 1024 + lane * 16);
    const u32x2 pm = *(const LAS u32x2*)(PM + (h * 2 + qo) * 512 + lane * 8);
    const bf16x8 vx = *(const LAS bf16x8*)(VS + (h * 4 + (wave >> 1)) * 1024 + lane * 16);
    acc = mfma32(vx, pt, acc);
    accm = mfma16(as_s4(vm[h]), as_s4(pm), accm);
  }
  acc += accm;
  const float gb = bflo(pk(gate, 0.f));
  const float og = bflo(pk(1.0f - gb, 0.f));
  const float bv[4] = {bflo(bf.x), bfhi(bf.x), bflo(bf.y), bfhi(bf.y)};
  float o[4];
#pragma unroll
  for (int k = 0; k < 4; ++k) {
    const float av = bflo(pk(acc[k], 0.f));
    o[k] = bflo(pk(gb * av, 0.f)) + bflo(pk(og * bv[k], 0.f));
  }
  const u32x2 ov = {pk(o[0], o[1]), pk(o[2], o[3])};
  *(u32x2*)(P.out + (size_t)(32 * s + 16 * qo + n) * DM + 16 * cb + 4 * g) = ov;
}

constexpr int NTASK = 32 * 17;
__device__ __forceinline__ void generic_wg(const Params& P, LAS unsigned char* lds) {
  int tmo = 0;
  LAS int* tk = (LAS int*)(lds + LDS_BYTES - 16);
  for (;;) {
    __syncthreads();
    if (threadIdx.x == 0) *tk = (int)__hip_atomic_fetch_add((GAS unsigned*)(P.ws + FL_Q), 1u, __ATOMIC_RELAXED, __HIP_MEMORY_SCOPE_AGENT);
    __syncthreads();
    const int t = __builtin_amdgcn_readfirstlane(*tk);
    if (t >= NTASK) break;
    int kind = 2, s = 0, c = 0;
    {
      int cnt = t, npre = 0, p = -1;
      for (;;) {
        const int lim = min(32, p < 0 ? 2 : (3 * (p + 1)) / 2 + 2);
        if (cnt < 3 * (lim - npre)) { const int pp = npre + cnt / 3, o = cnt % 3; kind = o == 0 ? 1 : 0; s = o == 0 ? pp : 2 * pp + o - 1; break; }
        cnt -= 3 * (lim - npre); npre = lim; ++p;
        if (cnt < 14) { kind = 2; s = 2 * p + (cnt >= 7 ? 1 : 0); c = cnt >= 7 ? cnt - 7 : cnt; break; }
        cnt -= 14;
      }
    }
    if (threadIdx.x < 64) STAMP(P, 1024 + 2 * t);
    if (kind == 0) task_sxx(P, s, lds, tmo);
    else if (kind == 1) task_ffn2(P, s, lds, tmo);
    else task_attn(P, s, c, lds, tmo);
    if (threadIdx.x < 64) STAMP(P, 1024 + 2 * t + 1);
  }
}

__global__ void __launch_bounds__(512, 2) main_kernel(Params P) {
  extern __shared__ __attribute__((aligned(16))) unsigned char smem[];
  LAS unsigned char* lds = (LAS unsigned char*)smem;
  const int b = blockIdx.x, r = b >> 3;
  const int x = (b + (int)*(const unsigned*)(P.ws + FL_ROT)) & 7;
  if (threadIdx.x < 64) STAMP(P, 512 + b);
  if (x < 4) {
    if (x == 0 || x == 2) {
      if (r < 16) {
        const int idx = (x >> 1) * 16 + r;
        const int wave = __builtin_amdgcn_readfirstlane(threadIdx.x >> 6);
        if (wave < 7) score_main(P, idx, wave, lds); else score_sx(P, idx, lds);
      }
      else if (x == 0 && r == 16) probe_pol(P);
      else if (r == 17 && WARM) warm_tlb(P);
    } else if (r < 14) {
      value_wg(P, (x >> 1) * 14 + r, lds);
    } else if (r == 17 && WARM) warm_tlb(P);
  } else {
    const int y = x - 4;
    if (r < 8) stream_wg<0>(P, y * 8 + r, lds);
    else if (r < 16) stream_wg<1>(P, y * 8 + r - 8, lds);
    else if (r < 20) stream_wg<2>(P, y * 4 + r - 16, lds);
    generic_wg(P, lds);
  }
}

}

extern "C" void kernel_launch(void* const* d_in, const int* in_sizes, int n_in, void* d_out, int out_size, void* d_ws, size_t ws_size,
                              hipStream_t stream) {
  Params P;
  P.x = (const unsigned short*)d_in[0];
  P.mem0 = (const unsigned short*)d_in[1];
  P.Wq = (const unsigned short*)d_in[2];
  P.Wk = (const unsigned short*)d_in[3];
  P.Wv = (const unsigned short*)d_in[4];
  P.Wo = (const unsigned short*)d_in[5];
  P.Wg = (const unsigned short*)d_in[8];
  P.Wu = (const unsigned short*)d_in[9];
  P.Wd = (const unsigned short*)d_in[10];
  P.gw = (const unsigned short*)d_in[11];
  P.out = (unsigned short*)d_out;
  const uintptr_t base = ((uintptr_t)d_ws + ((1u << 21) - 1)) & ~(uintptr_t)((1u << 21) - 1);
  P.ws = (unsigned char*)base;
  (void)hipFuncSetAttribute((const void*)prep_kernel, hipFuncAttributeMaxDynamicSharedMemorySize, 57344);
  (void)hipFuncSetAttribute((const void*)main_kernel, hipFuncAttributeMaxDynamicSharedMemorySize, LDS_BYTES);
  hipLaunchKernelGGL(prep_kernel, dim3(256), dim3(512), 57344, stream, P);
  hipLaunchKernelGGL(main_kernel, dim3(256), dim3(512), LDS_BYTES, stream, P);
}
```

```cpp
#include <hip/hip_runtime.h>
#include <stdint.h>

namespace {

typedef __bf16 bf16x2 __attribute__((ext_vector_type(2)));
typedef __bf16 bf16x8 __attribute__((ext_vector_type(8)));
typedef __bf16 bf16x16 __attribute__((ext_vector_type(16)));
typedef short s16x4 __attribute__((ext_vector_type(4)));
typedef float f32x4 __attribute__((ext_vector_type(4)));
typedef unsigned u32x4 __attribute__((ext_vector_type(4)));
typedef unsigned u32x2 __attribute__((ext_vector_type(2)));
#define LAS __attribute__((address_space(3)))
#define GAS __attribute__((address_space(1)))
typedef __amdgpu_buffer_rsrc_t rsrc_t;

constexpr unsigned SENT = 0xFFFFFFFFu;
constexpr int NSEG = 64;
constexpr int DM = 448;
constexpr int AUX_SC1 = 16;

constexpr unsigned SZ_W = 16u * 28 * 14 * 1024;
constexpr unsigned OFF_WK = 0;
constexpr unsigned OFF_WV = OFF_WK + SZ_W;
constexpr unsigned OFF_KX = OFF_WV + SZ_W;
constexpr unsigned KX_S = 16u * 2 * 14 * 1024;
constexpr unsigned OFF_VX = OFF_KX + 64u * KX_S;
constexpr unsigned VX_S = 16u * 28 * 1024;
constexpr unsigned OFF_KM = OFF_VX + 64u * VX_S;
constexpr unsigned KM_S = 16u * 14 * 512;
constexpr unsigned OFF_VM = OFF_KM + 64u * KM_S;
constexpr unsigned VM_S = 16u * 28 * 512;
constexpr unsigned OFF_WD = OFF_VM + 64u * VM_S;
constexpr unsigned OFF_HF = OFF_WD + 28u * 56 * 1024;
constexpr unsigned HF_S = 2u * 112 * 512;
constexpr unsigned OFF_BF = OFF_HF + 64u * HF_S;
constexpr unsigned BF_S = 2u * 28 * 512;
constexpr unsigned OFF_SXX = OFF_BF + 64u * BF_S;
constexpr unsigned SXX_S = 16u * 3 * 1024;
constexpr unsigned OFF_XF = OFF_SXX + 64u * SXX_S;
constexpr unsigned XF_S = 28u * 1024;
constexpr unsigned OFF_G = OFF_XF + 64u * XF_S;
constexpr unsigned OFF_FL = OFF_G + 8192;
constexpr unsigned FS = 128;
constexpr unsigned FL_T1K = OFF_FL;
constexpr unsigned FL_T1V = FL_T1K + 32 * FS;
constexpr unsigned FL_F1 = FL_T1V + 32 * FS;
constexpr unsigned FL_CH = FL_F1 + 16 * FS;
constexpr unsigned FL_T2 = FL_CH + 64 * FS;
constexpr unsigned FL_F2 = FL_T2 + 64 * FS;
constexpr unsigned FL_POL = FL_F2 + 64 * FS;
constexpr unsigned FL_ROT = FL_POL + FS;
constexpr unsigned FL_Q = FL_POL - FS;
constexpr unsigned FL_COUNT = 32 + 32 + 16 + 64 + 64 + 64 + 1;
constexpr unsigned OFF_DBG = OFF_FL + 65536;
constexpr unsigned OFF_MB = 112u << 20;
constexpr unsigned WS_USED = OFF_MB + (8u << 20);
static_assert(OFF_DBG + (2u << 20) <= OFF_MB, "workspace map");
constexpr unsigned K_SM = 0, K_SX = 32768, K_M = 65536, K_REP = 16384, K_PROBE = 98304;

constexpr unsigned SPIN_MAX = 40000u;
constexpr int LDS_BYTES = 144 * 1024;

#define TUNE 0
struct Params {
  const unsigned short* x;
  const unsigned short* mem0;
  const unsigned short* Wq;
  const unsigned short* Wk;
  const unsigned short* Wv;
  const unsigned short* Wo;
  const unsigned short* Wg;
  const unsigned short* Wu;
  const unsigned short* Wd;
  const unsigned short* gw;
  unsigned short* out;
  unsigned char* ws;
};

#define D_SX 6
#define D_SM 1
#define D_MV 7
#define D_MS 28
#define PROBEDBG 0
#define K1PLAIN 0
#define NEWSCHED 1
#define POLX 0
#define WARM 1
#define OVERLAP 1
#define D_GAP 5
#define D_BULK 4

__device__ __forceinline__ unsigned pk(float a, float b) {
  bf16x2 v = {(__bf16)a, (__bf16)b};
  return __builtin_bit_cast(unsigned, v);
}
__device__ __forceinline__ unsigned san(unsigned v) { return min(v, 0xFFFFFFFEu); }
__device__ __forceinline__ float bflo(unsigned v) { return __builtin_bit_cast(float, v << 16); }
__device__ __forceinline__ float bfhi(unsigned v) { return __builtin_bit_cast(float, v & 0xFFFF0000u); }
__device__ __forceinline__ float vmax(float a, float b) { return __builtin_elementwise_maximum(a, b); }
template <int N> __device__ __forceinline__ void delay_c() {
#pragma unroll 1
  for (int i = 0; i < N; ++i) __builtin_amdgcn_s_sleep(1);
}
__device__ __forceinline__ void delay_r(int n) {
#pragma unroll 1
  for (int i = 0; i < n; ++i) __builtin_amdgcn_s_sleep(1);
}
#define TUNE_DECL
#define DELAY(NAME, K) delay_c<NAME>()
#define TDP nullptr
__device__ __forceinline__ float max16(float v) {
  unsigned u = __builtin_bit_cast(unsigned, v);
  auto rr = __builtin_amdgcn_permlane16_swap(u, u, false, false);
  const unsigned r0 = rr[0], r1 = rr[1];
  return vmax(__builtin_bit_cast(float, r0), __builtin_bit_cast(float, r1));
}
__device__ __forceinline__ float max32(float v) {
  unsigned u = __builtin_bit_cast(unsigned, v);
  auto rr = __builtin_amdgcn_permlane32_swap(u, u, false, false);
  const unsigned r0 = rr[0], r1 = rr[1];
  return vmax(__builtin_bit_cast(float, r0), __builtin_bit_cast(float, r1));
}
__device__ __forceinline__ float sum16(float v) {
  unsigned u = __builtin_bit_cast(unsigned, v);
  auto rr = __builtin_amdgcn_permlane16_swap(u, u, false, false);
  const unsigned r0 = rr[0], r1 = rr[1];
  return __builtin_bit_cast(float, r0) + __builtin_bit_cast(float, r1);
}
__device__ __forceinline__ float sum32(float v) {
  unsigned u = __builtin_bit_cast(unsigned, v);
  auto rr = __builtin_amdgcn_permlane32_swap(u, u, false, false);
  const unsigned r0 = rr[0], r1 = rr[1];
  return __builtin_bit_cast(float, r0) + __builtin_bit_cast(float, r1);
}
__device__ __forceinline__ bf16x8 as_b8(u32x4 v) { return __builtin_bit_cast(bf16x8, v); }
__device__ __forceinline__ s16x4 as_s4(u32x2 v) { return __builtin_bit_cast(s16x4, v); }
__device__ __forceinline__ bf16x16 cat16(bf16x8 a, bf16x8 b) {
  return __builtin_shufflevector(a, b, 0, 1, 2, 3, 4, 5, 6, 7, 8, 9, 10, 11, 12, 13, 14, 15);
}
__device__ __forceinline__ f32x4 mfma32(bf16x8 a, bf16x8 b, f32x4 c) { return __builtin_amdgcn_mfma_f32_16x16x32_bf16(a, b, c, 0, 0, 0); }
__device__ __forceinline__ f32x4 mfma16(s16x4 a, s16x4 b, f32x4 c) { return __builtin_amdgcn_mfma_f32_16x16x16bf16_1k(a, b, c, 0, 0, 0); }
__device__ __forceinline__ f32x4 smf(bf16x8 a, bf16x16 b, f32x4 c, int idx) { return __builtin_amdgcn_smfmac_f32_16x16x64_bf16(a, b, c, idx, 0, 0); }
__device__ __forceinline__ u32x4 ld16(rsrc_t rs, unsigned v, unsigned s) { return __builtin_amdgcn_raw_buffer_load_b128(rs, v, s, AUX_SC1); }
__device__ __forceinline__ u32x2 ld8(rsrc_t rs, unsigned v, unsigned s) { return __builtin_amdgcn_raw_buffer_load_b64(rs, v, s, AUX_SC1); }
__device__ __forceinline__ unsigned ld4(rsrc_t rs, unsigned v, unsigned s) { return __builtin_amdgcn_raw_buffer_load_b32(rs, v, s, AUX_SC1); }
__device__ __forceinline__ void st16(u32x4 d, rsrc_t rs, unsigned v, unsigned s) {
  __builtin_amdgcn_raw_buffer_store_b128(d, rs, v, s, AUX_SC1);
  asm volatile("s_nop 1" : "+v"(d)::"memory");
}
__device__ __forceinline__ void st8(u32x2 d, rsrc_t rs, unsigned v, unsigned s) { __builtin_amdgcn_raw_buffer_store_b64(d, rs, v, s, AUX_SC1); }
__device__ __forceinline__ void st4(unsigned d, rsrc_t rs, unsigned v, unsigned s) { __builtin_amdgcn_raw_buffer_store_b32(d, rs, v, s, AUX_SC1); }
__device__ __forceinline__ rsrc_t ws_rsrc(unsigned char* ws) { return __builtin_amdgcn_make_buffer_rsrc(ws, 0, WS_USED, 0x00020000); }
__device__ __forceinline__ void glds16(const void* src, LAS unsigned char* dst) {
  __builtin_amdgcn_global_load_lds((const GAS unsigned*)src, (LAS unsigned*)dst, 16, 0, 0);
}
__device__ __forceinline__ u32x4 ws_desc(unsigned char* ws) {
  const unsigned long long a = (unsigned long long)ws;
  return u32x4{(unsigned)a, (unsigned)(a >> 32) & 0xFFFFu, WS_USED, 0x00020000u};
}
#define ALD16(dst, voff, desc, soff) asm volatile("buffer_load_dwordx4 %0, %1, %2, %3 offen sc1" : "=&v"(dst) : "v"(voff), "s"(desc), "s"(soff) : "memory")
#define ALD8(dst, voff, desc, soff) asm volatile("buffer_load_dwordx2 %0, %1, %2, %3 offen sc1" : "=&v"(dst) : "v"(voff), "s"(desc), "s"(soff) : "memory")
#define ALD4(dst, voff, desc, soff) asm volatile("buffer_load_dword %0, %1, %2, %3 offen sc1" : "=&v"(dst) : "v"(voff), "s"(desc), "s"(soff) : "memory")
#define KEEP(x) asm volatile("" ::"v"(x))
#define PINW(w) _Pragma("unroll") for (int k_ = 0; k_ < 7; ++k_) asm volatile("" : "+v"((w)[k_]))
#define BAR() do { asm volatile("s_waitcnt lgkmcnt(0)" ::: "memory"); __builtin_amdgcn_s_barrier(); asm volatile("" ::: "memory"); } while (0)
#define VMWAIT0() asm volatile("s_waitcnt vmcnt(0)" ::: "memory")
#define SCHED() __builtin_amdgcn_sched_barrier(0)
#define TIMING 0
#define LIGHT 0
#define LSTAMP(P, slot) do { } while (0)
#define DBG_DECL
#define DT(slot) do { } while (0)
#define DBG_END(s, last) do { } while (0)
#define DBG_OUT(P, wg, wave) do { } while (0)
#define STAMP(P, slot) do { } while (0)
#define CNT(P, slot, v) do { } while (0)

__device__ __forceinline__ bool any_sent4(u32x4 v) {
  unsigned m = max(max(v.x, v.y), max(v.z, v.w));
  return __builtin_amdgcn_ballot_w64(m == SENT) != 0ull;
}
__device__ __forceinline__ bool any_sent2(u32x2 a, u32x2 b) {
  unsigned m = max(max(a.x, a.y), max(b.x, b.y));
  return __builtin_amdgcn_ballot_w64(m == SENT) != 0ull;
}
__device__ __forceinline__ bool any_sent8(u32x4 a, u32x4 b) {
  unsigned m = max(max(max(a.x, a.y), max(a.z, a.w)), max(max(b.x, b.y), max(b.z, b.w)));
  return __builtin_amdgcn_ballot_w64(m == SENT) != 0ull;
}
__device__ __forceinline__ unsigned mbase(int s, int pol) {
  return OFF_MB + (unsigned)s * 131072u + (((unsigned)pol >> (s >> 4)) & 1u) * 8192u;
}
__device__ __forceinline__ int wait_flags2(rsrc_t rs, unsigned off0, unsigned off1, int need, int& tmo) {
  const int lane = threadIdx.x & 63;
  const unsigned vo = (lane & 1) ? off1 : off0;
  int lo = 0;
  for (unsigned sp = 0;; ++sp) {
    asm volatile("" ::: "memory");
    int v = (int)ld4(rs, vo, 0);
    lo = min(__builtin_amdgcn_readlane(v, 0), __builtin_amdgcn_readlane(v, 1));
    if (lo >= need) break;
    if (sp > SPIN_MAX || tmo) { tmo = 1; lo = NSEG + 1; break; }
    __builtin_amdgcn_s_sleep(4);
  }
  return lo;
}

__global__ void __launch_bounds__(512, 2) prep_kernel(Params P) {
  extern __shared__ __attribute__((aligned(16))) unsigned char smem[];
  LAS unsigned char* lds = (LAS unsigned char*)smem;
  const int tid = threadIdx.x, lane = tid & 63, wave = __builtin_amdgcn_readfirstlane(tid >> 6);
  const int b = blockIdx.x, xcd = b & 7, j = b >> 3;
  const int wh = xcd * 4 + (j >> 3), which = wh >> 4, h = wh & 15, rb = (j >> 2) & 1, cq = j & 3;
  const int n = lane & 15, g = lane >> 4;
  const rsrc_t rs = ws_rsrc(P.ws);
  if (b == 0 && wave == 0) { STAMP(P, 8); LSTAMP(P, 8); }
  if (wave == 0) LSTAMP(P, 2816 + b);

  if (which == 0) {
#pragma unroll
    for (int k = 0; k < 7; ++k) {
      const int fr = wave * 7 + k;
      const int ct = 7 * cq + (fr >> 3), ke = fr & 7;
      const int dp = 32 * (ct >> 1) + 8 * (n >> 2) + 4 * (ct & 1) + (n & 3);
      glds16(P.Wq + ((size_t)(dp * 16 + h) * 256 + 32 * ke + 8 * g), lds + fr * 1024);
    }
  } else {
#pragma unroll
    for (int k = 0; k < 7; ++k) {
      const int i = (wave * 7 + k) * 64 + lane;
      const int e = i / 14, ch = i - e * 14;
      glds16(P.Wo + ((size_t)(h * 256 + e) * 448 + 112 * cq + 8 * ch), lds + (wave * 7 + k) * 1024);
    }
  }
  if (wave == 7) {
    u32x4 xv[8];
    unsigned short wv[7][8];
    unsigned mv[4];
    if (b >= 8) {
#pragma unroll
      for (int k = 0; k < 8; ++k) {
        const int fr = min(b - 8 + 248 * k, 64 * 28 - 1), s = fr / 28, kq = fr - s * 28, q = kq >= 14 ? 1 : 0, k2 = kq - 14 * q;
        xv[k] = *(const u32x4*)(P.x + (size_t)(32 * s + 16 * q + n) * DM + 32 * k2 + 8 * g);
      }
#pragma unroll
      for (int k = 0; k < 7; ++k) {
        const int fr = min(b - 8 + 248 * k, 28 * 56 - 1), cb = fr / 56, u = fr - cb * 56;
#pragma unroll
        for (int jj = 0; jj < 8; ++jj) wv[k][jj] = P.Wd[(size_t)(32 * u + 16 * (jj >> 2) + 4 * g + (jj & 3)) * DM + 16 * cb + n];
      }
    } else if (b < 7) {
#pragma unroll
      for (int k = 0; k < 4; ++k) {
        const int i = k * 64 + lane, gg = (i >> 6) & 3, hi = (i >> 5) & 1, r = (i >> 2) & 7, p = i & 3;
        mv[k] = *(const unsigned*)(P.mem0 + r * DM + 64 * b + 16 * gg + 4 * p + 2 * hi);
      }
    }
    VMWAIT0();
    __syncthreads();
    if (b >= 8) {
#pragma unroll
      for (int k = 0; k < 8; ++k) {
        const int fr = b - 8 + 248 * k;
        if (fr < 64 * 28) st16(xv[k], rs, (unsigned)lane * 16u, OFF_XF + (unsigned)fr * 1024u);
      }
#pragma unroll
      for (int k = 0; k < 7; ++k) {
        const int fr = b - 8 + 248 * k;
        const u32x4 o = {wv[k][0] | ((unsigned)wv[k][1] << 16), wv[k][2] | ((unsigned)wv[k][3] << 16), wv[k][4] | ((unsigned)wv[k][5] << 16), wv[k][6] | ((unsigned)wv[k][7] << 16)};
        if (fr < 28 * 56) st16(o, rs, (unsigned)lane * 16u, OFF_WD + (unsigned)fr * 1024u);
      }
    } else if (b < 7) {
#pragma unroll
      for (int k = 0; k < 4; ++k)
#pragma unroll
        for (int c = 0; c < 4; ++c) st4(san(mv[k]), rs, (unsigned)(k * 64 + lane) * 4u, OFF_MB + K_M + (unsigned)c * 8192u + (unsigned)b * 1024u);
    }
  }
  bf16x8 a[2][8];
  const int kk = 7 * rb + (wave < 7 ? wave : 0);
  if (wave < 7) {
    const unsigned short* W1 = which ? P.Wv : P.Wk;
#pragma unroll
    for (int T = 0; T < 2; ++T) {
      const int d = 32 * kk + 8 * (n >> 2) + 4 * T + (n & 3);
      const unsigned short* p = W1 + ((size_t)(d * 16 + h) * 256 + 8 * g);
#pragma unroll
      for (int ke = 0; ke < 8; ++ke) a[T][ke] = *(const bf16x8*)(p + 32 * ke);
    }
    VMWAIT0();
    __syncthreads();
  }
  if (b == 0 && wave == 0) STAMP(P, 10);
  if (wave == 0) LSTAMP(P, 2560 + b);
  {
    const u32x4 sv = {SENT, SENT, SENT, SENT};
#pragma unroll
    for (int k = 0; k < 4; ++k) {
      const unsigned o = (unsigned)b * 32768u + (unsigned)(k * 512 + tid) * 16u;
      if (!(o >= K_M && o < K_M + 32768u)) st16(sv, rs, OFF_MB + o, 0);
    }
    if (b == 0 && tid == 0) *(unsigned*)(P.ws + FL_ROT) = (unsigned)__builtin_amdgcn_s_getreg(20 | (3 << 11)) & 7u;
    if (b == 3 && tid < (int)FL_COUNT) st4(tid == (int)FL_COUNT - 1 ? SENT : 0u, rs, OFF_FL + (unsigned)tid * FS, 0);
  }
  if (wave == 7) { LSTAMP(P, 2048 + b); return; }
  if (b == 0 && wave == 0) STAMP(P, 11);
  f32x4 acc[2][7];
#pragma unroll
  for (int c = 0; c < 7; ++c) { acc[0][c] = f32x4{0, 0, 0, 0}; acc[1][c] = f32x4{0, 0, 0, 0}; }
  if (which == 0) {
#pragma unroll
    for (int ke = 0; ke < 8; ++ke)
#pragma unroll
      for (int c = 0; c < 7; ++c) {
        const bf16x8 bb = *(const LAS bf16x8*)(lds + (c * 8 + ke) * 1024 + lane * 16);
        acc[0][c] = mfma32(a[0][ke], bb, acc[0][c]);
        acc[1][c] = mfma32(a[1][ke], bb, acc[1][c]);
      }
  } else {
    const LAS unsigned char* tb = lds + (8 * g + (n >> 2)) * 224 + (n & 3) * 8;
#pragma unroll
    for (int ke = 0; ke < 8; ++ke)
#pragma unroll
      for (int c = 0; c < 7; ++c) {
        typedef short v4s __attribute__((ext_vector_type(4)));
        const v4s lo = __builtin_amdgcn_ds_read_tr16_b64_v4i16((LAS v4s*)(tb + ke * 32 * 224 + c * 32));
        const v4s hi = __builtin_amdgcn_ds_read_tr16_b64_v4i16((LAS v4s*)(tb + ke * 32 * 224 + 4 * 224 + c * 32));
        const bf16x8 bb = __builtin_bit_cast(bf16x8, __builtin_shufflevector(lo, hi, 0, 1, 2, 3, 4, 5, 6, 7));
        acc[0][c] = mfma32(a[0][ke], bb, acc[0][c]);
        acc[1][c] = mfma32(a[1][ke], bb, acc[1][c]);
      }
  }
  const float sc = which ? 1.0f : 0.0625f * 1.4426950408889634f;
  const unsigned dst = (which ? OFF_WV : OFF_WK) + (unsigned)((h * 28 + 7 * cq) * 14 + kk) * 1024u;
#pragma unroll
  for (int c = 0; c < 7; ++c) {
    const f32x4 u = acc[0][c] * sc, v = acc[1][c] * sc;
    const u32x4 o = {pk(u[0], u[1]), pk(u[2], u[3]), pk(v[0], v[1]), pk(v[2], v[3])};
    st16(o, rs, (unsigned)lane * 16u, dst + (unsigned)c * 14u * 1024u);
  }
  if (b == 0 && wave == 0) { STAMP(P, 9); LSTAMP(P, 9); }
  if (wave == 0) LSTAMP(P, 2304 + b);
}

__device__ __forceinline__ u32x4 gather_m(const int* td_, u32x4 dsc, unsigned voff, unsigned so0, LAS unsigned char* dst, bool act, int& tmo, unsigned& nfail) {
  u32x4 va, vb;
  ALD16(va, voff, dsc, so0);
  const unsigned so1 = so0 + K_REP;
  if (OVERLAP) {
    DELAY(D_GAP, 4);
    ALD16(vb, voff, dsc, so1);
    asm volatile("s_waitcnt vmcnt(1)" : "+v"(va)::"memory");
  } else {
    vb = u32x4{0, 0, 0, 0};
    asm volatile("s_waitcnt vmcnt(0)" : "+v"(va)::"memory");
  }
  if (__builtin_expect(any_sent4(va), 0)) {
    if (TIMING) ++nfail;
    if (!OVERLAP) ALD16(vb, voff, dsc, so1);
    asm volatile("s_waitcnt vmcnt(0)" : "+v"(vb)::"memory");
    va = vb;
    for (unsigned sp = 2; any_sent4(va); ++sp) {
      asm volatile("" ::: "memory");
      const unsigned r = sp % 3u, so = r == 2u ? (so0 ^ 8192u) : so0 + r * K_REP;
      ALD16(va, voff, dsc, so);
      asm volatile("s_waitcnt vmcnt(0)" : "+v"(va)::"memory");
      if (sp > SPIN_MAX || tmo) { tmo = 1; break; }
    }
  }
  if (act) *(LAS u32x4*)dst = va;
  return vb;
}

__device__ __forceinline__ void probe_pol(const Params& P) {
  if (threadIdx.x >= 64) return;
  const rsrc_t rs = ws_rsrc(P.ws);
  __builtin_amdgcn_s_sleep(64);
  unsigned pol = 0;
  for (int round = 0; round < 3; ++round) {
    int dsum = 0;
#pragma unroll
    for (int p = 0; p < 4; ++p) {
      unsigned t[2];
#pragma unroll
      for (int cls = 0; cls < 2; ++cls) {
        const unsigned off = OFF_MB + (unsigned)(p * 16 + round) * 131072u + K_PROBE + (unsigned)cls * 8192u;
        const unsigned long long c0 = __builtin_amdgcn_s_memtime();
        unsigned v = ld4(rs, 0, off);
        asm volatile("s_waitcnt vmcnt(0)" : "+v"(v)::"memory");
        const unsigned long long c1 = __builtin_amdgcn_s_memtime();
        t[cls] = (unsigned)(c1 - c0);
      }
      const int d = max(-300, min(300, (int)t[0] - (int)t[1]));
      dsum += (p & 1) ? -d : d;
    }
    pol = 0;
#pragma unroll
    for (int p = 0; p < 4; ++p) pol |= (((dsum > 0) ? 1u : 0u) ^ (unsigned)(p & 1)) << p;
    if (dsum > 450 || dsum < -450) break;
  }
  if (threadIdx.x == 0) {
    st4(pol, rs, FL_POL, 0);
  }
}
__device__ __forceinline__ int get_pol(rsrc_t rs, int& tmo) {
  unsigned v;
  for (unsigned sp = 0;; ++sp) {
    asm volatile("" ::: "memory");
    v = (unsigned)__builtin_amdgcn_readfirstlane((int)ld4(rs, 0, FL_POL));
    if (v != SENT) break;
    if (sp > SPIN_MAX) { tmo = 1; v = 0; break; }
    __builtin_amdgcn_s_sleep(4);
  }
  return (int)v;
}

__device__ __forceinline__ void warm_tlb(const Params& P) {
  unsigned z = 0;
  asm volatile("" : "+v"(z));
  unsigned acc = 0;
  constexpr unsigned N1 = (OFF_WD - OFF_KX) >> 16, N2 = 8u << 4;
  for (unsigned i = threadIdx.x; i < N1 + N2 + 1; i += 512) {
    const unsigned off = i < N1 ? OFF_KX + (i << 16) : OFF_MB + ((i - N1) << 16) + K_PROBE + 4096;
    if (i < N1 + N2) acc += __hip_atomic_fetch_add((GAS unsigned*)(P.ws + off), z, __ATOMIC_RELAXED, __HIP_MEMORY_SCOPE_AGENT);
  }
  asm volatile("" ::"v"(acc));
}

__device__ __forceinline__ void score_main(const Params& P, int idx, int wave, LAS unsigned char* lds) {
  const int lane = threadIdx.x & 63;
  const int h = idx >> 1, q = idx & 1;
  const rsrc_t rs = ws_rsrc(P.ws);
  LAS unsigned char* img = lds;
  LAS float* red = (LAS float*)(lds + 8192);
  const int g = lane >> 4, n = lane & 15;
  const int idxv = (n & 8) ? 0xEEEE : 0x4444;
  s16x4 E;
#pragma unroll
  for (int jj = 0; jj < 4; ++jj) E[jj] = (((4 * g + jj) & 7) == n) ? (short)0x3F80 : (short)0;
  int tmo = 0;
  unsigned nfail = 0;
  if (idx == 0 && wave == 0) STAMP(P, 0);
  bf16x16 w0[7], w1[7];
  const int Pp = 7 * q + wave;
  {
    const unsigned char* b0 = P.ws + OFF_WK + ((size_t)(h * 28 + 2 * Pp) * 14) * 1024 + lane * 16;
#pragma unroll
    for (int ks = 0; ks < 7; ++ks) {
      w0[ks] = cat16(*(const bf16x8*)(b0 + (2 * ks) * 1024), *(const bf16x8*)(b0 + (2 * ks + 1) * 1024));
      w1[ks] = cat16(*(const bf16x8*)(b0 + (14 + 2 * ks) * 1024), *(const bf16x8*)(b0 + (15 + 2 * ks) * 1024));
    }
  }
  const int pol = get_pol(rs, tmo) ^ POLX;
  const unsigned gloc = (unsigned)(wave * 56 + (lane < 56 ? lane : 55)) * 16u;
  const unsigned gvoff = gloc + K_M;
  LAS unsigned char* gdst = img + gloc;
  const LAS unsigned char* afp = img + lane * 16;
  const LAS unsigned char* mp = img + (Pp >> 1) * 1024 + (2 * (Pp & 1) + (g >> 1)) * 256 + (n & 7) * 16 + (g & 1) * 8;
  const unsigned mpa = (unsigned)(size_t)mp;
  LAS float* redw = red + wave * 64 + n * 8 + 4 * g;
  const unsigned xfv = OFF_XF + (unsigned)Pp * 1024u + lane * 16, xfv1 = xfv + 14 * 1024;
  LAS float* red2 = (LAS float*)(lds + 12288);
  LAS float* red2w = red2 + wave * 256 + (g & 1) * 64 + n * 4;
  const LAS float* red2r = red2 + lane * 4;
  const unsigned sxmv = OFF_KM + (unsigned)idx * 1024u + lane * 16;
  const unsigned smv = K_SM + (unsigned)idx * 256u + lane * 4u;
  PINW(w0); PINW(w1);
  const u32x4 dsc = ws_desc(P.ws);
  u32x4 xf0, xf1;
  { const unsigned so = 0; ALD16(xf0, xfv, dsc, so); ALD16(xf1, xfv1, dsc, so); }
  TUNE_DECL
  DBG_DECL
  for (int s = 0; s < NSEG; ++s) {
    const unsigned mb = mbase(s, pol);
    if (s > 0) DELAY(D_MS, 0);
    DT(0);
    const u32x4 pend = gather_m(TDP, dsc, gvoff, mb, gdst, lane < 56, tmo, nfail);
    DT(1);
    BAR();
    DT(2);
    f32x4 c0, c1;
    u32x4 mf;
    {
      u32x2 mlo, mhi;
      asm volatile("ds_read2_b32 %0, %2 offset0:0 offset1:32\n\tds_read2_b32 %1, %2 offset0:1 offset1:33" : "=&v"(mlo), "=&v"(mhi) : "v"(mpa) : "memory");
      bf16x8 a[7];
#pragma unroll
      for (int ks = 0; ks < 7; ++ks) a[ks] = *(const LAS bf16x8*)(afp + ks * 1024);
      SCHED();
      c0 = f32x4{0, 0, 0, 0}; c1 = f32x4{0, 0, 0, 0};
#pragma unroll
      for (int ks = 0; ks < 7; ++ks) {
        c0 = smf(a[ks], w0[ks], c0, idxv);
        c1 = smf(a[ks], w1[ks], c1, idxv);
      }
      SCHED();
      asm volatile("s_waitcnt lgkmcnt(0)" : "+v"(mlo), "+v"(mhi)::"memory");
      mf = u32x4{mlo.x, mlo.y, mhi.x, mhi.y};
    }
    SCHED();
    const u32x2 t0 = {pk(c0[0], c0[1]), pk(c0[2], c0[3])}, t1 = {pk(c1[0], c1[1]), pk(c1[2], c1[3])};
    const f32x4 z = {0, 0, 0, 0};
    SCHED();
    const f32x4 y0 = mfma16(as_s4(t0), E, z), y1 = mfma16(as_s4(t1), E, z);
    SCHED();
    const u32x4 kf = {pk(y0[0], y0[1]), pk(y0[2], y0[3]), pk(y1[0], y1[1]), pk(y1[2], y1[3])};
    const f32x4 S = mfma32(as_b8(kf), as_b8(mf), z);
    if (n < 8 && g < 2) *(LAS f32x4*)redw = S;
    VMWAIT0();
    KEEP(pend);
    DT(3);
    BAR();
    DT(4);
    if (wave == 0 && s < NSEG - 1) {
      const float v0 = red[lane], v1 = red[64 + lane], v2 = red[128 + lane], v3 = red[192 + lane], v4 = red[256 + lane], v5 = red[320 + lane], v6 = red[384 + lane];
      const float v = ((v0 + v1) + (v2 + v3)) + ((v4 + v5) + v6);
      const unsigned o = san(__builtin_bit_cast(unsigned, v));
      st4(o, rs, smv, mb);
      st4(o, rs, smv + K_REP, mb);
      st4(o, rs, smv, mb ^ 8192u);
    }
    DT(5);
    if (idx == 0 && wave == 2 && (s & 7) == 0) LSTAMP(P, 64 + s);
    DBG_END(s, 5);
    asm volatile("" : "+v"(xf0), "+v"(xf1));
    {
      const f32x4 d0 = mfma32(as_b8(kf), as_b8(xf0), z), d1 = mfma32(as_b8(kf), as_b8(xf1), z);
      if (g < 2) { *(LAS f32x4*)red2w = d0; *(LAS f32x4*)(red2w + 128) = d1; }
    }
    BAR();
    if (wave == 1) {
      f32x4 v = *(const LAS f32x4*)red2r;
#pragma unroll
      for (int w = 1; w < 7; ++w) v += *(const LAS f32x4*)(red2r + w * 256);
      st16(__builtin_bit_cast(u32x4, v), rs, sxmv, (unsigned)s * KM_S);
    }
    if (s < NSEG - 1) DELAY(D_BULK, 5);
    { const unsigned so = (unsigned)min(s + 1, NSEG - 1) * XF_S; ALD16(xf0, xfv, dsc, so); ALD16(xf1, xfv1, dsc, so); }
  }
  DBG_OUT(P, idx, wave);
  VMWAIT0();
  BAR();
  CNT(P, 0, nfail);
  if (idx == 0 && wave == 0) { STAMP(P, 1); LSTAMP(P, 1); }
}

__device__ __forceinline__ void score_sx(const Params& P, int idx, LAS unsigned char* lds) {
  const int lane = threadIdx.x & 63;
  const int wave = 7;
  const int h = idx >> 1, q = idx & 1;
  const rsrc_t rs = ws_rsrc(P.ws);
  LAS unsigned char* img = lds;
  const int g = lane >> 4, n = lane & 15;
  const int idxv = (n & 8) ? 0xEEEE : 0x4444;
  s16x4 E;
#pragma unroll
  for (int jj = 0; jj < 4; ++jj) E[jj] = (((4 * g + jj) & 7) == n) ? (short)0x3F80 : (short)0;
  int tmo = 0;
  unsigned nfail = 0;
  const int pol = get_pol(rs, tmo) ^ POLX;
  if (idx == 0) STAMP(P, 2);
  const unsigned f0 = FL_T1K + (unsigned)(h * 2) * FS, f1 = f0 + FS;
  int known = wait_flags2(rs, f0, f1, 1, tmo);
  if (idx == 0) STAMP(P, 3);
  int fl = 0;
  const unsigned flv = (lane & 1) ? f1 : f0;
  u32x4 kx[14];
  const unsigned kxv = OFF_KX + (unsigned)((h * 2 + q) * 14) * 1024u + lane * 16;
  const u32x4 dsc = ws_desc(P.ws);
  const unsigned kxa[4] = {kxv, kxv + 4096, kxv + 8192, kxv + 12288};
#define KXLD(k, so) asm volatile("buffer_load_dwordx4 %0, %1, %2, %3 offen offset:%c4 sc1" : "=&v"(kx[k]) : "v"(kxa[(k) >> 2]), "s"(dsc), "s"(so), "i"(((k) & 3) * 1024) : "memory")
  {
    const unsigned so = 0;
    KXLD(0, so); KXLD(1, so); KXLD(2, so); KXLD(3, so); KXLD(4, so); KXLD(5, so); KXLD(6, so);
    KXLD(7, so); KXLD(8, so); KXLD(9, so); KXLD(10, so); KXLD(11, so); KXLD(12, so); KXLD(13, so);
  }
  const unsigned gloc = (unsigned)(wave * 56 + (lane < 56 ? lane : 55)) * 16u;
  const unsigned gvoff = gloc + K_M;
  LAS unsigned char* gdst = img + gloc;
  const LAS unsigned char* afp = img + lane * 16;
  const unsigned sxv = K_SX + (unsigned)idx * 256u + (unsigned)(g * 8 + (n & 7)) * 8u;
  const unsigned chf = FL_CH + (unsigned)idx * FS;
  __builtin_amdgcn_s_setprio(3);
  TUNE_DECL
  for (int s = 0; s < NSEG; ++s) {
    const unsigned mb = mbase(s, pol);
    if (s > 0) DELAY(D_MS, 0);
    const u32x4 pend = gather_m(TDP, dsc, gvoff, mb, gdst, lane < 56, tmo, nfail);
    BAR();
    {
      bf16x8 a[7];
#pragma unroll
      for (int ks = 0; ks < 7; ++ks) a[ks] = *(const LAS bf16x8*)(afp + ks * 1024);
      SCHED();
      f32x4 c0 = {0, 0, 0, 0};
#pragma unroll
      for (int ks = 0; ks < 7; ++ks) c0 = smf(a[ks], cat16(as_b8(kx[2 * ks]), as_b8(kx[2 * ks + 1])), c0, idxv);
      VMWAIT0();
      KEEP(pend);
      const u32x2 t0 = {pk(c0[0], c0[1]), pk(c0[2], c0[3])};
      const f32x4 z = {0, 0, 0, 0};
      const f32x4 y0 = mfma16(as_s4(t0), E, z);
      const u32x2 o = {san(pk(y0[0], y0[1])), san(pk(y0[2], y0[3]))};
      if (n < 8 && s < NSEG - 1) {
        st8(o, rs, sxv, mb);
        st8(o, rs, sxv + K_REP, mb);
        st8(o, rs, sxv, mb ^ 8192u);
      }
      if (lane == 0) st4((unsigned)s, rs, chf, 0);
      SCHED();
    }
    BAR();
    BAR();
    if (s < NSEG - 1) DELAY(D_BULK, 5);
    const int sn = min(s + 1, NSEG - 2);
    asm volatile("" : "+v"(fl));
    known = max(known, min(__builtin_amdgcn_readlane(fl, 0), __builtin_amdgcn_readlane(fl, 1)));
    if (__builtin_expect(known <= sn, 0)) { known = wait_flags2(rs, f0, f1, sn + 1, tmo); CNT(P, 4, 1); }
    { const unsigned z = 0; ALD4(fl, flv, dsc, z); }
    {
      const unsigned so = (unsigned)sn * KX_S;
      KXLD(0, so); KXLD(1, so); KXLD(2, so); KXLD(3, so); KXLD(4, so); KXLD(5, so); KXLD(6, so);
      KXLD(7, so); KXLD(8, so); KXLD(9, so); KXLD(10, so); KXLD(11, so); KXLD(12, so); KXLD(13, so);
    }
    SCHED();
  }
  VMWAIT0();
  BAR();
  if (lane == 0) st4((unsigned)NSEG, rs, chf, 0);
  CNT(P, 0, nfail);
}

__device__ __forceinline__ void value_wg(const Params& P, int cb, LAS unsigned char* lds) {
  const int tid = threadIdx.x, lane = tid & 63, wave = __builtin_amdgcn_readfirstlane(tid >> 6);
  const rsrc_t rs = ws_rsrc(P.ws);
  LAS unsigned char* img = lds;
  LAS float* red = (LAS float*)(lds + 8192);
  const int g = lane >> 4, n = lane & 15, sel = n >> 3, i = n & 7;
  const int idxv = (n & 8) ? 0xEEEE : 0x4444;
  const int h0 = 2 * wave, hs = h0 + sel;
  int tmo = 0;
  unsigned nfail = 0, nfx = 0, nfm = 0;
  bf16x16 w0[7], w1[7];
  {
    const unsigned char* b0 = P.ws + OFF_WV + ((size_t)(h0 * 28 + cb) * 14) * 1024 + lane * 16;
    const unsigned char* b1 = b0 + (size_t)28 * 14 * 1024;
#pragma unroll
    for (int ks = 0; ks < 7; ++ks) {
      w0[ks] = cat16(*(const bf16x8*)(b0 + (2 * ks) * 1024), *(const bf16x8*)(b0 + (2 * ks + 1) * 1024));
      w1[ks] = cat16(*(const bf16x8*)(b1 + (2 * ks) * 1024), *(const bf16x8*)(b1 + (2 * ks + 1) * 1024));
    }
  }
  const int pol = get_pol(rs, tmo) ^ POLX;
  const unsigned fa = FL_T1V + (unsigned)(h0 * 2 + (cb >= 14 ? 1 : 0)) * FS, fb = fa + 2 * FS;
  int known = 0;
  const unsigned vxv = OFF_VX + (unsigned)(h0 * 28 + cb) * 1024u + lane * 16, vxv1 = vxv + 28 * 1024;
  const unsigned flv = (lane & 1) ? fb : fa;
  const unsigned gloc = (unsigned)(wave * 56 + (lane < 56 ? lane : 55)) * 16u;
  const unsigned gvoff = gloc + K_M;
  LAS unsigned char* gdst = img + gloc;
  const LAS unsigned char* afp = img + lane * 16;
  const unsigned vmv = OFF_VM + (unsigned)(h0 * 28 + cb) * 512u + lane * 8;
  const unsigned sxv = K_SX + (unsigned)(hs * 2) * 256u + (unsigned)(g * 8 + i) * 8u;
  const unsigned smv = K_SM + (unsigned)(hs * 2) * 256u + (unsigned)(i * 8 + 4 * (g & 1)) * 4u;
  LAS float* redw = red + wave * 128 + i * 16 + 4 * g;
  const LAS float* redr = red + ((lane >> 2) & 7) * 16 + 4 * (lane & 3) + 2 * (lane >> 5);
  const unsigned mv = K_M + (unsigned)cb * 256u + lane * 4u;
  const unsigned chf = FL_CH + (unsigned)(32 + cb) * FS;
  u32x2 vm0 = {0, 0}, vm1 = {0, 0};
  PINW(w0); PINW(w1);
  const u32x4 dsc = ws_desc(P.ws);
  TUNE_DECL
  DBG_DECL
  for (int s = 0; s < NSEG - 1; ++s) {
    const unsigned mb = mbase(s, pol), mbn = mbase(s + 1, pol);
    if (s > 0) DELAY(D_MV, 1);
    DT(0);
    const u32x4 pend = gather_m(TDP, dsc, gvoff, mb, gdst, lane < 56, tmo, nfail);
    DT(1);
    BAR();
    if (__builtin_expect(known <= s, 0)) { known = wait_flags2(rs, fa, fb, s + 1, tmo); CNT(P, 5, 1); }
    bf16x8 a[7];
#pragma unroll
    for (int ks = 0; ks < 7; ++ks) a[ks] = *(const LAS bf16x8*)(afp + ks * 1024);
    u32x4 vx0, vx1;
    {
      const unsigned so = (unsigned)s * VX_S;
      ALD16(vx0, vxv, dsc, so);
      ALD16(vx1, vxv1, dsc, so);
    }
    st8(vm0, rs, vmv, (unsigned)max(s - 1, 0) * VM_S);
    st8(vm1, rs, vmv + 28 * 512, (unsigned)max(s - 1, 0) * VM_S);
    if (wave == 1 && lane == 0) st4((unsigned)max(s - 1, 0), rs, chf, 0);
    int fl;
    { const unsigned z = 0; ALD4(fl, flv, dsc, z); }
    SCHED();
    f32x4 c0 = {0, 0, 0, 0}, c1 = {0, 0, 0, 0};
#pragma unroll
    for (int ks = 0; ks < 7; ++ks) {
      c0 = smf(a[ks], w0[ks], c0, idxv);
      c1 = smf(a[ks], w1[ks], c1, idxv);
    }
    vm0 = u32x2{pk(c0[0], c0[1]), pk(c0[2], c0[3])};
    vm1 = u32x2{pk(c1[0], c1[1]), pk(c1[2], c1[3])};
    SCHED();
    DT(2);
    DELAY(D_SX, 2);
    u32x2 x0, x1, y0, y1;
    u32x4 ua, ub, wa, wb;
    const unsigned sxv1 = sxv + 256, smv1 = smv + 256, mb1 = mb + K_REP;
    ALD8(x0, sxv, dsc, mb);
    ALD8(x1, sxv1, dsc, mb);
    DELAY(D_GAP, 4);
    ALD8(y0, sxv, dsc, mb1);
    ALD8(y1, sxv1, dsc, mb1);
    DELAY(D_SM, 3);
    ALD16(ua, smv, dsc, mb);
    ALD16(ub, smv1, dsc, mb);
    asm volatile("s_waitcnt vmcnt(4)" : "+v"(x0), "+v"(x1), "+v"(vx0), "+v"(vx1), "+v"(fl)::"memory");
    KEEP(pend);
    known = max(known, min(__builtin_amdgcn_readlane(fl, 0), __builtin_amdgcn_readlane(fl, 1)));
    if (__builtin_expect(any_sent2(x0, x1), 0)) {
      if (TIMING) ++nfx;
      asm volatile("s_waitcnt vmcnt(2)" : "+v"(y0), "+v"(y1)::"memory");
      x0 = y0; x1 = y1;
      for (unsigned sp = 2; any_sent2(x0, x1); ++sp) {
        asm volatile("" ::: "memory");
        const unsigned r = sp % 3u, so = r == 2u ? (mb ^ 8192u) : mb + r * K_REP;
        ALD8(x0, sxv, dsc, so);
        ALD8(x1, sxv1, dsc, so);
        asm volatile("s_waitcnt vmcnt(0)" : "+v"(x0), "+v"(x1)::"memory");
        if (sp > SPIN_MAX || tmo) { tmo = 1; break; }
      }
    }
    DT(3);
    SCHED();
    float t[8] = {bflo(x0.x), bfhi(x0.x), bflo(x0.y), bfhi(x0.y), bflo(x1.x), bfhi(x1.x), bflo(x1.y), bfhi(x1.y)};
    float m1 = vmax(vmax(vmax(t[0], t[1]), vmax(t[2], t[3])), vmax(vmax(t[4], t[5]), vmax(t[6], t[7])));
    m1 = max32(max16(m1));
    float l1 = 0.f;
#pragma unroll
    for (int k = 0; k < 8; ++k) { t[k] = __builtin_amdgcn_exp2f(t[k] - m1); l1 += t[k]; }
    SCHED();
    ALD16(wa, smv, dsc, mb1);
    ALD16(wb, smv1, dsc, mb1);
    SCHED();
    l1 = sum32(sum16(l1));
    const u32x4 pb = {pk(t[0], t[1]), pk(t[2], t[3]), pk(t[4], t[5]), pk(t[6], t[7])};
    const f32x4 z = {0, 0, 0, 0};
    const f32x4 ax0 = mfma32(as_b8(vx0), as_b8(pb), z), ax1 = mfma32(as_b8(vx1), as_b8(pb), z);
    const f32x4 ax = sel ? ax1 : ax0;
    SCHED();
    DT(4);
    asm volatile("s_waitcnt vmcnt(2)" : "+v"(ua), "+v"(ub)::"memory");
    KEEP(y0); KEEP(y1);
    if (__builtin_expect(any_sent8(ua, ub), 0)) {
      if (TIMING) ++nfm;
      asm volatile("s_waitcnt vmcnt(0)" : "+v"(wa), "+v"(wb)::"memory");
      ua = wa; ub = wb;
      for (unsigned sp = 2; any_sent8(ua, ub); ++sp) {
        asm volatile("" ::: "memory");
        const unsigned r = sp % 3u, so = r == 2u ? (mb ^ 8192u) : mb + r * K_REP;
        ALD16(ua, smv, dsc, so);
        ALD16(ub, smv1, dsc, so);
        asm volatile("s_waitcnt vmcnt(0)" : "+v"(ua), "+v"(ub)::"memory");
        if (sp > SPIN_MAX || tmo) { tmo = 1; break; }
      }
    }
    DT(5);
    const f32x4 sm = __builtin_bit_cast(f32x4, ua) + __builtin_bit_cast(f32x4, ub);
    float m2 = vmax(vmax(sm[0], sm[1]), vmax(sm[2], sm[3]));
    m2 = max16(m2);
    const float M = vmax(m1, m2);
    const float al = __builtin_amdgcn_exp2f(m1 - M);
    const float e0 = __builtin_amdgcn_exp2f(sm[0] - M), e1 = __builtin_amdgcn_exp2f(sm[1] - M), e2 = __builtin_amdgcn_exp2f(sm[2] - M), e3 = __builtin_amdgcn_exp2f(sm[3] - M);
    const u32x2 pm = {pk(e0, e1), pk(e2, e3)};
    const f32x4 cx = ax * al;
    const f32x4 r0 = mfma16(as_s4(vm0), as_s4(pm), cx), r1 = mfma16(as_s4(vm1), as_s4(pm), cx);
    const float l2 = sum16((e0 + e1) + (e2 + e3));
    const float rl = __builtin_amdgcn_rcpf(al * l1 + l2);
    f32x4 r = (sel ? r1 : r0) * rl;
#pragma unroll
    for (int k = 0; k < 4; ++k) {
      const float rk = r[k];
      const int o = __builtin_amdgcn_update_dpp(0, __builtin_bit_cast(int, rk), 0x128, 0xf, 0xf, true);
      r[k] += __builtin_bit_cast(float, o);
    }
    if (sel == 0) *(LAS f32x4*)redw = r;
    BAR();
    DT(6);
    if (wave == 0) {
      float a0[8], a1[8];
#pragma unroll
      for (int w = 0; w < 8; ++w) { a0[w] = redr[w * 128]; a1[w] = redr[w * 128 + 1]; }
      const float s0 = ((a0[0] + a0[1]) + (a0[2] + a0[3])) + ((a0[4] + a0[5]) + (a0[6] + a0[7]));
      const float s1 = ((a1[0] + a1[1]) + (a1[2] + a1[3])) + ((a1[4] + a1[5]) + (a1[6] + a1[7]));
      const unsigned o = san(pk(s0, s1));
      st4(o, rs, mv, mbn);
      st4(o, rs, mv + K_REP, mbn);
      st4(o, rs, mv, mbn ^ 8192u);
      asm volatile("s_waitcnt vmcnt(3)" ::: "memory");
    } else {
      VMWAIT0();
    }
    KEEP(wa); KEEP(wb);
    DT(7);
    DBG_END(s, 7);
  }
  DBG_OUT(P, 32 + cb, wave);
  {
    const int s = NSEG - 1;
    const unsigned mb = mbase(s, pol);
    DELAY(D_MV, 1);
    const u32x4 pend = gather_m(TDP, dsc, gvoff, mb, gdst, lane < 56, tmo, nfail);
    BAR();
    st8(vm0, rs, vmv, (unsigned)(s - 1) * VM_S);
    st8(vm1, rs, vmv + 28 * 512, (unsigned)(s - 1) * VM_S);
    f32x4 c0 = {0, 0, 0, 0}, c1 = {0, 0, 0, 0};
#pragma unroll
    for (int ks = 0; ks < 7; ++ks) {
      const bf16x8 a = *(const LAS bf16x8*)(afp + ks * 1024);
      c0 = smf(a, w0[ks], c0, idxv);
      c1 = smf(a, w1[ks], c1, idxv);
    }
    vm0 = u32x2{pk(c0[0], c0[1]), pk(c0[2], c0[3])};
    vm1 = u32x2{pk(c1[0], c1[1]), pk(c1[2], c1[3])};
    st8(vm0, rs, vmv, (unsigned)s * VM_S);
    st8(vm1, rs, vmv + 28 * 512, (unsigned)s * VM_S);
    VMWAIT0();
    KEEP(pend);
    BAR();
    if (wave == 1 && lane == 0) st4((unsigned)NSEG, rs, chf, 0);
    CNT(P, 1, nfail); CNT(P, 2, nfx); CNT(P, 3, nfm);
    if (cb == 0 && wave == 0) STAMP(P, 4);
  }
}

__device__ __forceinline__ void stage_x(const Params& P, int s, LAS unsigned char* buf, int lane, int first, int count) {
  const unsigned char* p = P.ws + OFF_XF + (size_t)s * XF_S + lane * 16;
  for (int k = first; k < first + count; ++k) glds16(p + k * 1024, buf + k * 1024);
}

template <int MODE>
__device__ __forceinline__ void stream_wg(const Params& P, int id, LAS unsigned char* lds) {
  const int tid = threadIdx.x, lane = tid & 63, wave = __builtin_amdgcn_readfirstlane(tid >> 6);
  const rsrc_t rs = ws_rsrc(P.ws);
  const int n = lane & 15, g = lane >> 4;
  bf16x8 w0[14], w1[14];
  if (MODE == 2) {
#pragma unroll 1
    for (int m = 0; m < 2; ++m) {
      const unsigned short* W = m ? P.Wu : P.Wg;
      for (int k = wave; k < 98; k += 8) {
        const int i = k * 64 + lane, d = i / 14, ch = i - d * 14;
        glds16(W + (size_t)d * 1792 + 112 * id + 8 * ch, lds + k * 1024);
      }
      VMWAIT0();
      __syncthreads();
      if (wave < 7) {
        const LAS unsigned char* tb = lds + (8 * g + (n >> 2)) * 224 + (n & 3) * 8 + wave * 32;
#pragma unroll
        for (int kk = 0; kk < 14; ++kk) {
          typedef short v4s __attribute__((ext_vector_type(4)));
          const v4s lo = __builtin_amdgcn_ds_read_tr16_b64_v4i16((LAS v4s*)(tb + kk * 32 * 224));
          const v4s hi = __builtin_amdgcn_ds_read_tr16_b64_v4i16((LAS v4s*)(tb + kk * 32 * 224 + 4 * 224));
          const bf16x8 f = __builtin_bit_cast(bf16x8, __builtin_shufflevector(lo, hi, 0, 1, 2, 3, 4, 5, 6, 7));
          if (m == 0) w0[kk] = f; else w1[kk] = f;
        }
      }
      __syncthreads();
    }
  } else if (wave < 7) {
    const int h = id >> 1, hq = id & 1;
    const unsigned char* b0 = P.ws + (MODE ? OFF_WV : OFF_WK) + ((size_t)(h * 28 + 14 * hq + 2 * wave) * 14) * 1024 + lane * 16;
#pragma unroll
    for (int kk = 0; kk < 14; ++kk) { w0[kk] = *(const bf16x8*)(b0 + kk * 1024); w1[kk] = *(const bf16x8*)(b0 + (14 + kk) * 1024); }
  }
  const unsigned fl = (MODE == 0 ? FL_T1K : MODE == 1 ? FL_T1V : FL_F1) + (unsigned)id * FS;
  if (wave == 7) {
    stage_x(P, 0, lds, lane, 0, 28);
    stage_x(P, 1, lds + 28672, lane, 0, 28);
    asm volatile("s_waitcnt vmcnt(28)" ::: "memory");
    int nb = 2;
    for (int s = 0; s < NSEG; ++s) {
      BAR();
      if (id == 0) STAMP(P, 192 + MODE * 64 + s);
      if (s + 2 < NSEG) { stage_x(P, s + 2, lds + nb * 28672, lane, 0, 28); asm volatile("s_waitcnt vmcnt(28)" ::: "memory"); }
      else VMWAIT0();
      nb = nb == 2 ? 0 : nb + 1;
    }
    BAR();
    if (lane == 0) st4((unsigned)NSEG, rs, fl, 0);
  } else {
    int cbuf = 0;
    for (int s = 0; s < NSEG; ++s) {
      BAR();
      if (wave == 0 && s > 0 && lane == 0) st4((unsigned)(s <= 2 ? s : s - 1), rs, fl, 0);
      const LAS unsigned char* xb = lds + cbuf * 28672 + lane * 16;
      cbuf = cbuf == 2 ? 0 : cbuf + 1;
      f32x4 c00 = {0, 0, 0, 0}, c01 = {0, 0, 0, 0}, c10 = {0, 0, 0, 0}, c11 = {0, 0, 0, 0};
      bf16x8 xa[3], xc[3];
      xa[0] = *(const LAS bf16x8*)(xb); xc[0] = *(const LAS bf16x8*)(xb + 14 * 1024);
      xa[1] = *(const LAS bf16x8*)(xb + 1024); xc[1] = *(const LAS bf16x8*)(xb + 15 * 1024);
#pragma unroll
      for (int kk = 0; kk < 14; ++kk) {
        if (kk + 2 < 14) { xa[(kk + 2) % 3] = *(const LAS bf16x8*)(xb + (kk + 2) * 1024); xc[(kk + 2) % 3] = *(const LAS bf16x8*)(xb + (16 + kk) * 1024); }
        SCHED();
        const bf16x8 x0 = xa[kk % 3], x1 = xc[kk % 3];
        if (MODE == 1) { c00 = mfma32(x0, w0[kk], c00); c01 = mfma32(x0, w1[kk], c01); c10 = mfma32(x1, w0[kk], c10); c11 = mfma32(x1, w1[kk], c11); }
        else { c00 = mfma32(w0[kk], x0, c00); c01 = mfma32(w1[kk], x0, c01); c10 = mfma32(w0[kk], x1, c10); c11 = mfma32(w1[kk], x1, c11); }
        SCHED();
      }
      if (MODE == 2) {
#pragma unroll
        for (int q = 0; q < 2; ++q) {
          const f32x4 cg = q ? c10 : c00, cu = q ? c11 : c01;
          float hv[4];
#pragma unroll
          for (int k = 0; k < 4; ++k) {
            const float gg = cg[k];
            hv[k] = gg * __builtin_amdgcn_rcpf(1.0f + __builtin_amdgcn_exp2f(-1.4426950408889634f * gg)) * cu[k];
          }
          const u32x2 o = {pk(hv[0], hv[1]), pk(hv[2], hv[3])};
          st8(o, rs, OFF_HF + (unsigned)(q * 112 + 7 * id + wave) * 512u + lane * 8, (unsigned)s * HF_S);
        }
      } else if (MODE == 0) {
        const int h = id >> 1, Pp = 7 * (id & 1) + wave;
        const u32x4 o0 = {pk(c00[0], c00[1]), pk(c00[2], c00[3]), pk(c01[0], c01[1]), pk(c01[2], c01[3])};
        const u32x4 o1 = {pk(c10[0], c10[1]), pk(c10[2], c10[3]), pk(c11[0], c11[1]), pk(c11[2], c11[3])};
        st16(o0, rs, OFF_KX + (unsigned)((h * 2 + 0) * 14 + Pp) * 1024u + lane * 16, (unsigned)s * KX_S);
        st16(o1, rs, OFF_KX + (unsigned)((h * 2 + 1) * 14 + Pp) * 1024u + lane * 16, (unsigned)s * KX_S);
      } else {
        const int h = id >> 1, cb = 14 * (id & 1) + 2 * wave;
        const u32x4 o0 = {pk(c00[0], c00[1]), pk(c00[2], c00[3]), pk(c10[0], c10[1]), pk(c10[2], c10[3])};
        const u32x4 o1 = {pk(c01[0], c01[1]), pk(c01[2], c01[3]), pk(c11[0], c11[1]), pk(c11[2], c11[3])};
        st16(o0, rs, OFF_VX + (unsigned)(h * 28 + cb) * 1024u + lane * 16, (unsigned)s * VX_S);
        st16(o1, rs, OFF_VX + (unsigned)(h * 28 + cb + 1) * 1024u + lane * 16, (unsigned)s * VX_S);
      }
      if (s < 2) VMWAIT0(); else asm volatile("s_waitcnt vmcnt(2)" ::: "memory");
    }
    VMWAIT0();
    BAR();
  }
}

template <int SLEEP>
__device__ __forceinline__ void wait_lanes(rsrc_t rs, unsigned voff, int need, int& tmo) {
  for (unsigned sp = 0;; ++sp) {
    asm volatile("" ::: "memory");
    const int v = (int)ld4(rs, voff, 0);
    if (__builtin_amdgcn_ballot_w64(v < need) == 0ull) break;
    if (sp > SPIN_MAX || tmo) { tmo = 1; break; }
    __builtin_amdgcn_s_sleep(SLEEP);
  }
}

__device__ __forceinline__ void task_sxx(const Params& P, int s, LAS unsigned char* lds, int& tmo) {
  int tid = threadIdx.x;
  asm volatile("" : "+v"(tid));
  const int lane = tid & 63, wave = __builtin_amdgcn_readfirstlane(tid >> 6);
  const rsrc_t rs = ws_rsrc(P.ws);
  __syncthreads();
  stage_x(P, s, lds, lane, wave * 4, wave < 7 ? 4 : 0);
  if (wave == 0) wait_lanes<24>(rs, FL_T1K + (unsigned)(lane & 31) * FS, s + 1, tmo);
  BAR();
  const unsigned kv = lane * 16;
  const unsigned so = OFF_KX + (unsigned)(2 * wave * 2 * 14) * 1024u + (unsigned)s * KX_S;
  u32x4 ka[2][7], kb[2][7];
#pragma unroll
  for (int k = 0; k < 7; ++k) { ka[0][k] = ld16(rs, kv, so + k * 1024); kb[0][k] = ld16(rs, kv, so + (14 + k) * 1024); }
  asm volatile("s_waitcnt vmcnt(14)" ::: "memory");
  __syncthreads();
  const LAS unsigned char* xb = lds + lane * 16;
  f32x4 c00 = {0, 0, 0, 0}, c01 = {0, 0, 0, 0}, c11 = {0, 0, 0, 0};
#pragma unroll
  for (int c = 0; c < 4; ++c) {
    if (c + 1 < 4) {
      const int hn = (c + 1) >> 1, kn = ((c + 1) & 1) * 7;
#pragma unroll
      for (int k = 0; k < 7; ++k) { ka[(c + 1) & 1][k] = ld16(rs, kv, so + (hn * 28 + kn + k) * 1024); kb[(c + 1) & 1][k] = ld16(rs, kv, so + (hn * 28 + 14 + kn + k) * 1024); }
    }
    SCHED();
#pragma unroll
    for (int k = 0; k < 7; ++k) {
      const int kk = (c & 1) * 7 + k;
      const bf16x8 x0 = *(const LAS bf16x8*)(xb + kk * 1024), x1 = *(const LAS bf16x8*)(xb + (14 + kk) * 1024);
      c00 = mfma32(as_b8(ka[c & 1][k]), x0, c00);
      c01 = mfma32(as_b8(ka[c & 1][k]), x1, c01);
      c11 = mfma32(as_b8(kb[c & 1][k]), x1, c11);
    }
    if (c & 1) {
      const unsigned o = OFF_SXX + (unsigned)((2 * wave + (c >> 1)) * 3) * 1024u + (unsigned)s * SXX_S;
      st16(__builtin_bit_cast(u32x4, c00), rs, kv, o);
      st16(__builtin_bit_cast(u32x4, c01), rs, kv, o + 1024);
      st16(__builtin_bit_cast(u32x4, c11), rs, kv, o + 2048);
      c00 = f32x4{0, 0, 0, 0}; c01 = f32x4{0, 0, 0, 0}; c11 = f32x4{0, 0, 0, 0};
    }
    SCHED();
  }
  VMWAIT0();
  __syncthreads();
  if (tid == 0) st4(1u, rs, FL_T2 + (unsigned)s * FS, 0);
}

__device__ __forceinline__ void task_ffn2(const Params& P, int sp, LAS unsigned char* lds, int& tmo) {
  int tid = threadIdx.x;
  asm volatile("" : "+v"(tid));
  const int lane = tid & 63, wave = __builtin_amdgcn_readfirstlane(tid >> 6);
  const rsrc_t rs = ws_rsrc(P.ws);
  __syncthreads();
  const unsigned char* wd = P.ws + OFF_WD + (size_t)(4 * (wave < 7 ? wave : 0)) * 56 * 1024 + lane * 16;
  bf16x8 wf[4][4];
#pragma unroll
  for (int uu = 0; uu < 4; ++uu)
#pragma unroll
    for (int c = 0; c < 4; ++c) wf[uu][c] = *(const bf16x8*)(wd + (size_t)(c * 56 + uu) * 1024);
  if (wave == 0) wait_lanes<24>(rs, FL_F1 + (unsigned)(lane & 15) * FS, 2 * sp + 2, tmo);
  BAR();
  f32x4 acc[4][4];
#pragma unroll
  for (int c = 0; c < 4; ++c)
#pragma unroll
    for (int q = 0; q < 4; ++q) acc[c][q] = f32x4{0, 0, 0, 0};
#pragma unroll 1
  for (int half = 0; half < 2; ++half) {
    if (half) __syncthreads();
    {
      u32x4 v[14];
#pragma unroll
      for (int k = 0; k < 14; ++k) {
        const int pc = wave * 14 + k, tile = pc / 28, f2 = pc - tile * 28;
        v[k] = ld16(rs, lane * 16, OFF_HF + (unsigned)(2 * sp + (tile >> 1)) * HF_S + (unsigned)((tile & 1) * 112 + 56 * half + 2 * f2) * 512u);
      }
#pragma unroll
      for (int k = 0; k < 14; ++k) *(LAS u32x4*)(lds + (wave * 14 + k) * 1024 + lane * 16) = v[k];
    }
    __syncthreads();
    if (wave < 7) {
      const LAS unsigned char* hb = lds + lane * 8;
#pragma unroll 1
      for (int u0 = 0; u0 < 28; u0 += 4) {
        const int ug = 28 * half + u0;
        const int un = ug + 4 < 56 ? ug + 4 : ug;
#pragma unroll
        for (int uu = 0; uu < 4; ++uu) {
          const int u = u0 + uu;
          u32x4 hf[4];
#pragma unroll
          for (int q = 0; q < 4; ++q) {
            const u32x2 lo = *(const LAS u32x2*)(hb + (q * 56 + 2 * u) * 512), hi = *(const LAS u32x2*)(hb + (q * 56 + 2 * u + 1) * 512);
            hf[q] = u32x4{lo.x, lo.y, hi.x, hi.y};
          }
#pragma unroll
          for (int q = 0; q < 4; ++q)
#pragma unroll
            for (int c = 0; c < 4; ++c) acc[c][q] = mfma32(wf[uu][c], as_b8(hf[q]), acc[c][q]);
          SCHED();
#pragma unroll
          for (int c = 0; c < 4; ++c) wf[uu][c] = *(const bf16x8*)(wd + (size_t)(c * 56 + un + uu) * 1024);
          SCHED();
        }
      }
    } else if (half == 0) {
      const unsigned short* xp = P.x + (size_t)(64 * sp + lane) * DM;
      float acc1 = 0.f;
#pragma unroll 7
      for (int k = 0; k < 56; ++k) {
        const u32x4 xv = *(const u32x4*)(xp + 8 * k), gv = *(const u32x4*)(P.gw + 8 * k);
        acc1 += bflo(xv.x) * bflo(gv.x) + bfhi(xv.x) * bfhi(gv.x) + bflo(xv.y) * bflo(gv.y) + bfhi(xv.y) * bfhi(gv.y) +
                bflo(xv.z) * bflo(gv.z) + bfhi(xv.z) * bfhi(gv.z) + bflo(xv.w) * bflo(gv.w) + bfhi(xv.w) * bfhi(gv.w);
      }
      const float gate = __builtin_amdgcn_rcpf(1.0f + __builtin_amdgcn_exp2f(-1.4426950408889634f * acc1));
      st4(__builtin_bit_cast(unsigned, gate), rs, (unsigned)lane * 4u, OFF_G + (unsigned)(64 * sp) * 4u);
    }
  }
  if (wave < 7) {
#pragma unroll
    for (int c = 0; c < 4; ++c)
#pragma unroll
      for (int q = 0; q < 4; ++q) {
        const u32x2 o = {pk(acc[c][q][0], acc[c][q][1]), pk(acc[c][q][2], acc[c][q][3])};
        st8(o, rs, lane * 8, OFF_BF + (unsigned)((q & 1) * 28 + 4 * wave + c) * 512u + (unsigned)(2 * sp + (q >> 1)) * BF_S);
      }
  }
  VMWAIT0();
  __syncthreads();
  if (tid == 0) st4(1u, rs, FL_F2 + (unsigned)sp * FS, 0);
}

template <int Q>
__device__ __forceinline__ void attn_softmax(const f32x4 sm, const f32x4 ta, const f32x4 tb, int g, int n, u32x4& pt, u32x2& pm) {
  const float NEG = -3.0e38f;
  float t[8];
#pragma unroll
  for (int k = 0; k < 4; ++k) {
    const bool causal = (4 * g + k) <= n;
    t[k] = Q ? ta[k] : (causal ? ta[k] : NEG);
    t[4 + k] = Q ? (causal ? tb[k] : NEG) : NEG;
  }
  float m = vmax(vmax(vmax(sm[0], sm[1]), vmax(sm[2], sm[3])), vmax(vmax(t[0], t[1]), vmax(t[2], t[3])));
  if (Q) m = vmax(m, vmax(vmax(t[4], t[5]), vmax(t[6], t[7])));
  m = max32(max16(m));
  float e[4], l = 0.f;
#pragma unroll
  for (int k = 0; k < 4; ++k) { e[k] = __builtin_amdgcn_exp2f(sm[k] - m); l += e[k]; }
  l *= 0.5f;
#pragma unroll
  for (int k = 0; k < (Q ? 8 : 4); ++k) { t[k] = __builtin_amdgcn_exp2f(t[k] - m); l += t[k]; }
  l = sum32(sum16(l));
  const float rl = __builtin_amdgcn_rcpf(l);
  pt = u32x4{pk(t[0] * rl, t[1] * rl), pk(t[2] * rl, t[3] * rl), Q ? pk(t[4] * rl, t[5] * rl) : 0u, Q ? pk(t[6] * rl, t[7] * rl) : 0u};
  pm = u32x2{pk(e[0] * rl, e[1] * rl), pk(e[2] * rl, e[3] * rl)};
}

__device__ __forceinline__ void task_attn(const Params& P, int s, int cbg, LAS unsigned char* lds, int& tmo) {
  int tid = threadIdx.x;
  asm volatile("" : "+v"(tid));
  const int lane = tid & 63, wave = __builtin_amdgcn_readfirstlane(tid >> 6);
  const rsrc_t rs = ws_rsrc(P.ws);
  const int n = lane & 15, g = lane >> 4;
  LAS unsigned char* PT = lds + 8192;
  LAS unsigned char* PM = PT + 32768;
  LAS unsigned char* VS = PM + 16384;
  __syncthreads();
  {
    const unsigned fo = lane < 32 ? FL_T1V + (unsigned)lane * FS : (lane & 1) ? FL_T2 + (unsigned)s * FS : FL_F2 + (unsigned)(s >> 1) * FS;
    if (wave == 0) wait_lanes<24>(rs, fo, lane < 32 ? s + 1 : 1, tmo);
    BAR();
  }
  const int cbl = wave >> 1, hh8 = wave & 1, cb = 4 * cbg + cbl, qo = hh8;
  f32x4 sxx[2][3];
  u32x2 bf;
  float gate;
  {
    u32x4 vx[8];
#pragma unroll
    for (int k = 0; k < 8; ++k) vx[k] = ld16(rs, lane * 16, OFF_VX + (unsigned)((2 * wave + (k >> 2)) * 28 + 4 * cbg + (k & 3)) * 1024u + (unsigned)s * VX_S);
#pragma unroll
    for (int hh = 0; hh < 2; ++hh)
#pragma unroll
      for (int k = 0; k < 3; ++k) sxx[hh][k] = __builtin_bit_cast(f32x4, ld16(rs, lane * 16, OFF_SXX + (unsigned)((2 * wave + hh) * 3 + k) * 1024u + (unsigned)s * SXX_S));
    bf = ld8(rs, lane * 8, OFF_BF + (unsigned)(qo * 28 + cb) * 512u + (unsigned)s * BF_S);
    gate = __builtin_bit_cast(float, ld4(rs, (unsigned)n * 4u, OFF_G + (unsigned)(32 * s + 16 * qo) * 4u));
#pragma unroll
    for (int k = 0; k < 8; ++k) *(LAS u32x4*)(VS + ((2 * wave + (k >> 2)) * 4 + (k & 3)) * 1024 + lane * 16) = vx[k];
  }
  VMWAIT0();
  __syncthreads();
  if (s == NSEG - 1 && cbg == 0 && wave == 0) STAMP(P, 15);
  if (wave == 0) {
    const unsigned vo = FL_CH + (unsigned)(lane < 60 ? lane : 0) * FS;
    for (unsigned sp = 0;; ++sp) {
      asm volatile("" ::: "memory");
      const int v = (int)ld4(rs, vo, 0);
      if (__builtin_amdgcn_ballot_w64(v < s + 1) == 0ull) break;
      if (sp > SPIN_MAX || tmo) { tmo = 1; break; }
      const int v0 = __builtin_amdgcn_readfirstlane(v);
      const int far = min(s - v0 - 1, 16);
      for (int k = 0; k < far; ++k) __builtin_amdgcn_s_sleep(48);
      __builtin_amdgcn_s_sleep(4);
    }
  }
  BAR();
  if (s == NSEG - 1 && cbg == 0 && wave == 0) STAMP(P, 16);
  f32x4 smq[2][2];
  u32x2 vm[8];
  {
    u32x4 sp[2][2][2];
#pragma unroll
    for (int hh = 0; hh < 2; ++hh)
#pragma unroll
      for (int q = 0; q < 2; ++q)
#pragma unroll
        for (int hq = 0; hq < 2; ++hq)
          sp[hh][q][hq] = ld16(rs, (unsigned)((g & 1) * 16 + n) * 16u, OFF_KM + (unsigned)((2 * wave + hh) * 2 + hq) * 1024u + (unsigned)q * 512u + (unsigned)s * KM_S);
#pragma unroll
    for (int h = 0; h < 8; ++h) vm[h] = ld8(rs, lane * 8, OFF_VM + (unsigned)((8 * hh8 + h) * 28 + cb) * 512u + (unsigned)s * VM_S);
    SCHED();
#pragma unroll
    for (int hh = 0; hh < 2; ++hh)
#pragma unroll
      for (int q = 0; q < 2; ++q) smq[hh][q] = __builtin_bit_cast(f32x4, sp[hh][q][0]) + __builtin_bit_cast(f32x4, sp[hh][q][1]);
  }
  if (s == NSEG - 1 && cbg == 0 && wave == 0) STAMP(P, 17);
#pragma unroll
  for (int hh = 0; hh < 2; ++hh) {
    const int h = 2 * wave + hh;
    u32x4 pt;
    u32x2 pm;
    attn_softmax<0>(smq[hh][0], sxx[hh][0], sxx[hh][2], g, n, pt, pm);
    *(LAS u32x4*)(PT + (h * 2 + 0) * 1024 + lane * 16) = pt;
    *(LAS u32x2*)(PM + (h * 2 + 0) * 512 + lane * 8) = pm;
    attn_softmax<1>(smq[hh][1], sxx[hh][1], sxx[hh][2], g, n, pt, pm);
    *(LAS u32x4*)(PT + (h * 2 + 1) * 1024 + lane * 16) = pt;
    *(LAS u32x2*)(PM + (h * 2 + 1) * 512 + lane * 8) = pm;
  }
  __syncthreads();
  if (s == NSEG - 1 && cbg == 0 && wave == 0) STAMP(P, 18);
  f32x4 at[2] = {{0, 0, 0, 0}, {0, 0, 0, 0}}, am[2] = {{0, 0, 0, 0}, {0, 0, 0, 0}};
#pragma unroll
  for (int k = 0; k < 8; ++k) {
    const int h = 8 * hh8 + k;
    const bf16x8 vx = *(const LAS bf16x8*)(VS + (h * 4 + cbl) * 1024 + lane * 16);
#pragma unroll
    for (int q = 0; q < 2; ++q) {
      const bf16x8 pt = *(const LAS bf16x8*)(PT + (h * 2 + q) * 1024 + lane * 16);
      const u32x2 pm = *(const LAS u32x2*)(PM + (h * 2 + q) * 512 + lane * 8);
      at[q] = mfma32(vx, pt, at[q]);
      am[q] = mfma16(as_s4(vm[k]), as_s4(pm), am[q]);
    }
  }
  LAS f32x4* ex = (LAS f32x4*)lds;
  ex[(wave ^ 1) * 64 + lane] = hh8 ? at[0] + am[0] : at[1] + am[1];
  __syncthreads();
  const f32x4 acc = (hh8 ? at[1] + am[1] : at[0] + am[0]) + ex[wave * 64 + lane];
  const float gb = bflo(pk(gate, 0.f));
  const float og = bflo(pk(1.0f - gb, 0.f));
  const float bv[4] = {bflo(bf.x), bfhi(bf.x), bflo(bf.y), bfhi(bf.y)};
  float o[4];
#pragma unroll
  for (int k = 0; k < 4; ++k) {
    const float av = bflo(pk(acc[k], 0.f));
    o[k] = bflo(pk(gb * av, 0.f)) + bflo(pk(og * bv[k], 0.f));
  }
  const u32x2 ov = {pk(o[0], o[1]), pk(o[2], o[3])};
  *(u32x2*)(P.out + (size_t)(32 * s + 16 * qo + n) * DM + 16 * cb + 4 * g) = ov;
}

constexpr int NTASK = 32 * 17;
__device__ __forceinline__ void generic_wg(const Params& P, LAS unsigned char* lds) {
  int tmo = 0;
  LAS int* tk = (LAS int*)(lds + LDS_BYTES - 16);
  for (;;) {
    __syncthreads();
    if (threadIdx.x == 0) *tk = (int)__hip_atomic_fetch_add((GAS unsigned*)(P.ws + FL_Q), 1u, __ATOMIC_RELAXED, __HIP_MEMORY_SCOPE_AGENT);
    __syncthreads();
    const int t = __builtin_amdgcn_readfirstlane(*tk);
    if (t >= NTASK) { if (threadIdx.x < 64) LSTAMP(P, 768 + blockIdx.x); break; }
    int kind = 2, s = 0, c = 0;
    {
      int cnt = t, npre = 0, p = -1;
      for (;;) {
        const int lim = min(32, p < 0 ? 2 : (3 * (p + 1)) / 2 + 2);
        if (cnt < 3 * (lim - npre)) { const int pp = npre + cnt / 3, o = cnt % 3; kind = o == 0 ? 1 : 0; s = o == 0 ? pp : 2 * pp + o - 1; break; }
        cnt -= 3 * (lim - npre); npre = lim; ++p;
        if (cnt < 14) { kind = 2; s = 2 * p + (cnt >= 7 ? 1 : 0); c = cnt >= 7 ? cnt - 7 : cnt; break; }
        cnt -= 14;
      }
    }
    if (threadIdx.x < 64) STAMP(P, 1024 + 2 * t);
    if (kind == 0) task_sxx(P, s, lds, tmo);
    else if (kind == 1) task_ffn2(P, s, lds, tmo);
    else task_attn(P, s, c, lds, tmo);
    if (threadIdx.x < 64) STAMP(P, 1024 + 2 * t + 1);
  }
}

__global__ void __launch_bounds__(512, 2) main_kernel(Params P) {
  extern __shared__ __attribute__((aligned(16))) unsigned char smem[];
  LAS unsigned char* lds = (LAS unsigned char*)smem;
  const int b = blockIdx.x, r = b >> 3;
  const int x = (b + (int)*(const unsigned*)(P.ws + FL_ROT)) & 7;
  if (threadIdx.x < 64) { STAMP(P, 512 + b); LSTAMP(P, 512 + b); }
  if (x < 4) {
    if (x == 0 || x == 2) {
      if (r < 16) {
        const int idx = (x >> 1) * 16 + r;
        const int wave = __builtin_amdgcn_readfirstlane(threadIdx.x >> 6);
        if (wave < 7) score_main(P, idx, wave, lds); else score_sx(P, idx, lds);
      }
      else if (x == 0 && r == 16) probe_pol(P);
      else if (r == 17 && WARM) warm_tlb(P);
    } else if (r < 14) {
      value_wg(P, (x >> 1) * 14 + r, lds);
    } else if (r == 17 && WARM) warm_tlb(P);
  } else {
    const int y = x - 4;
    if (r < 8) stream_wg<0>(P, y * 8 + r, lds);
    else if (r < 16) stream_wg<1>(P, y * 8 + r - 8, lds);
    else if (r < 20) stream_wg<2>(P, y * 4 + r - 16, lds);
    generic_wg(P, lds);
  }
}

}

extern "C" void kernel_launch(void* const* d_in, const int* in_sizes, int n_in, void* d_out, int out_size, void* d_ws, size_t ws_size,
                              hipStream_t stream) {
  Params P;
  P.x = (const unsigned short*)d_in[0];
  P.mem0 = (const unsigned short*)d_in[1];
  P.Wq = (const unsigned short*)d_in[2];
  P.Wk = (const unsigned short*)d_in[3];
  P.Wv = (const unsigned short*)d_in[4];
  P.Wo = (const unsigned short*)d_in[5];
  P.Wg = (const unsigned short*)d_in[8];
  P.Wu = (const unsigned short*)d_in[9];
  P.Wd = (const unsigned short*)d_in[10];
  P.gw = (const unsigned short*)d_in[11];
  P.out = (unsigned short*)d_out;
  const uintptr_t base = ((uintptr_t)d_ws + ((1u << 21) - 1)) & ~(uintptr_t)((1u << 21) - 1);
  P.ws = (unsigned char*)base;
  (void)hipFuncSetAttribute((const void*)prep_kernel, hipFuncAttributeMaxDynamicSharedMemorySize, 57344);
  (void)hipFuncSetAttribute((const void*)main_kernel, hipFuncAttributeMaxDynamicSharedMemorySize, LDS_BYTES);
  hipLaunchKernelGGL(prep_kernel, dim3(256), dim3(512), 57344, stream, P);
  hipLaunchKernelGGL(main_kernel, dim3(256), dim3(512), LDS_BYTES, stream, P);
}
```

```cpp
#include <hip/hip_runtime.h>
#include <stdint.h>

namespace {

typedef __bf16 bf16x2 __attribute__((ext_vector_type(2)));
typedef __bf16 bf16x8 __attribute__((ext_vector_type(8)));
typedef __bf16 bf16x16 __attribute__((ext_vector_type(16)));
typedef short s16x4 __attribute__((ext_vector_type(4)));
typedef float f32x4 __attribute__((ext_vector_type(4)));
typedef unsigned u32x4 __attribute__((ext_vector_type(4)));
typedef unsigned u32x2 __attribute__((ext_vector_type(2)));
#define LAS __attribute__((address_space(3)))
#define GAS __attribute__((address_space(1)))
typedef __amdgpu_buffer_rsrc_t rsrc_t;

constexpr unsigned SENT = 0xFFFFFFFFu;
constexpr int NSEG = 64;
constexpr int DM = 448;
constexpr int AUX_SC1 = 16;

constexpr unsigned SZ_W = 16u * 28 * 14 * 1024;
constexpr unsigned OFF_WK = 0;
constexpr unsigned OFF_WV = OFF_WK + SZ_W;
constexpr unsigned OFF_KX = OFF_WV + SZ_W;
constexpr unsigned KX_S = 16u * 2 * 14 * 1024;
constexpr unsigned OFF_VX = OFF_KX + 64u * KX_S;
constexpr unsigned VX_S = 16u * 28 * 1024;
constexpr unsigned OFF_KM = OFF_VX + 64u * VX_S;
constexpr unsigned KM_S = 16u * 14 * 512;
constexpr unsigned OFF_VM = OFF_KM + 64u * KM_S;
constexpr unsigned VM_S = 16u * 28 * 512;
constexpr unsigned OFF_WD = OFF_VM + 64u * VM_S;
constexpr unsigned OFF_HF = OFF_WD + 28u * 56 * 1024;
constexpr unsigned HF_S = 2u * 112 * 512;
constexpr unsigned OFF_BF = OFF_HF + 64u * HF_S;
constexpr unsigned BF_S = 2u * 28 * 512;
constexpr unsigned OFF_SXX = OFF_BF + 64u * BF_S;
constexpr unsigned SXX_S = 16u * 3 * 1024;
constexpr unsigned OFF_XF = OFF_SXX + 64u * SXX_S;
constexpr unsigned XF_S = 28u * 1024;
constexpr unsigned OFF_G = OFF_XF + 64u * XF_S;
constexpr unsigned OFF_FL = OFF_G + 8192;
constexpr unsigned FS = 128;
constexpr unsigned FL_T1K = OFF_FL;
constexpr unsigned FL_T1V = FL_T1K + 32 * FS;
constexpr unsigned FL_F1 = FL_T1V + 32 * FS;
constexpr unsigned FL_CH = FL_F1 + 16 * FS;
constexpr unsigned FL_T2 = FL_CH + 64 * FS;
constexpr unsigned FL_F2 = FL_T2 + 64 * FS;
constexpr unsigned FL_POL = FL_F2 + 64 * FS;
constexpr unsigned FL_ROT = FL_POL + FS;
constexpr unsigned FL_Q = FL_POL - FS;
constexpr unsigned FL_COUNT = 32 + 32 + 16 + 64 + 64 + 64 + 1;
constexpr unsigned OFF_DBG = OFF_FL + 65536;
constexpr unsigned OFF_MB = 112u << 20;
constexpr unsigned WS_USED = OFF_MB + (8u << 20);
static_assert(OFF_DBG + (2u << 20) <= OFF_MB, "workspace map");
constexpr unsigned K_SM = 0, K_SX = 32768, K_M = 65536, K_REP = 16384, K_PROBE = 98304;

constexpr unsigned SPIN_MAX = 40000u;
constexpr int LDS_BYTES = 144 * 1024;

#define TUNE 0
struct Params {
  const unsigned short* x;
  const unsigned short* mem0;
  const unsigned short* Wq;
  const unsigned short* Wk;
  const unsigned short* Wv;
  const unsigned short* Wo;
  const unsigned short* Wg;
  const unsigned short* Wu;
  const unsigned short* Wd;
  const unsigned short* gw;
  unsigned short* out;
  unsigned char* ws;
};

#define D_SX 12
#define D_SM 2
#define D_MV 7
#define D_MS 28
#define PROBEDBG 0
#define K1PLAIN 0
#define NEWSCHED 1
#define POLX 0
#define OVERLAP 1
#define D_GAP 5
#define L2HELP 1
#define VPRIO 0
#define D_G2 0
#define D_BULK 4

__device__ __forceinline__ unsigned pk(float a, float b) {
  bf16x2 v = {(__bf16)a, (__bf16)b};
  return __builtin_bit_cast(unsigned, v);
}
__device__ __forceinline__ unsigned san(unsigned v) { return min(v, 0xFFFFFFFEu); }
__device__ __forceinline__ float bflo(unsigned v) { return __builtin_bit_cast(float, v << 16); }
__device__ __forceinline__ float bfhi(unsigned v) { return __builtin_bit_cast(float, v & 0xFFFF0000u); }
__device__ __forceinline__ float vmax(float a, float b) { return __builtin_elementwise_maximum(a, b); }
template <int N> __device__ __forceinline__ void delay_c() {
#pragma unroll 1
  for (int i = 0; i < N; ++i) __builtin_amdgcn_s_sleep(1);
}
__device__ __forceinline__ void delay_r(int n) {
#pragma unroll 1
  for (int i = 0; i < n; ++i) __builtin_amdgcn_s_sleep(1);
}
#define TUNE_DECL
#define DELAY(NAME, K) delay_c<NAME>()
#define TDP nullptr
__device__ __forceinline__ float max16(float v) {
  unsigned u = __builtin_bit_cast(unsigned, v);
  auto rr = __builtin_amdgcn_permlane16_swap(u, u, false, false);
  const unsigned r0 = rr[0], r1 = rr[1];
  return vmax(__builtin_bit_cast(float, r0), __builtin_bit_cast(float, r1));
}
__device__ __forceinline__ float max32(float v) {
  unsigned u = __builtin_bit_cast(unsigned, v);
  auto rr = __builtin_amdgcn_permlane32_swap(u, u, false, false);
  const unsigned r0 = rr[0], r1 = rr[1];
  return vmax(__builtin_bit_cast(float, r0), __builtin_bit_cast(float, r1));
}
__device__ __forceinline__ float sum16(float v) {
  unsigned u = __builtin_bit_cast(unsigned, v);
  auto rr = __builtin_amdgcn_permlane16_swap(u, u, false, false);
  const unsigned r0 = rr[0], r1 = rr[1];
  return __builtin_bit_cast(float, r0) + __builtin_bit_cast(float, r1);
}
__device__ __forceinline__ float sum32(float v) {
  unsigned u = __builtin_bit_cast(unsigned, v);
  auto rr = __builtin_amdgcn_permlane32_swap(u, u, false, false);
  const unsigned r0 = rr[0], r1 = rr[1];
  return __builtin_bit_cast(float, r0) + __builtin_bit_cast(float, r1);
}
__device__ __forceinline__ bf16x8 as_b8(u32x4 v) { return __builtin_bit_cast(bf16x8, v); }
__device__ __forceinline__ s16x4 as_s4(u32x2 v) { return __builtin_bit_cast(s16x4, v); }
__device__ __forceinline__ bf16x16 cat16(bf16x8 a, bf16x8 b) {
  return __builtin_shufflevector(a, b, 0, 1, 2, 3, 4, 5, 6, 7, 8, 9, 10, 11, 12, 13, 14, 15);
}
__device__ __forceinline__ f32x4 mfma32(bf16x8 a, bf16x8 b, f32x4 c) { return __builtin_amdgcn_mfma_f32_16x16x32_bf16(a, b, c, 0, 0, 0); }
__device__ __forceinline__ f32x4 mfma16(s16x4 a, s16x4 b, f32x4 c) { return __builtin_amdgcn_mfma_f32_16x16x16bf16_1k(a, b, c, 0, 0, 0); }
__device__ __forceinline__ f32x4 smf(bf16x8 a, bf16x16 b, f32x4 c, int idx) { return __builtin_amdgcn_smfmac_f32_16x16x64_bf16(a, b, c, idx, 0, 0); }
__device__ __forceinline__ u32x4 ld16(rsrc_t rs, unsigned v, unsigned s) { return __builtin_amdgcn_raw_buffer_load_b128(rs, v, s, AUX_SC1); }
__device__ __forceinline__ u32x2 ld8(rsrc_t rs, unsigned v, unsigned s) { return __builtin_amdgcn_raw_buffer_load_b64(rs, v, s, AUX_SC1); }
__device__ __forceinline__ unsigned ld4(rsrc_t rs, unsigned v, unsigned s) { return __builtin_amdgcn_raw_buffer_load_b32(rs, v, s, AUX_SC1); }
__device__ __forceinline__ void st16(u32x4 d, rsrc_t rs, unsigned v, unsigned s) {
  __builtin_amdgcn_raw_buffer_store_b128(d, rs, v, s, AUX_SC1);
  asm volatile("s_nop 1" : "+v"(d)::"memory");
}
__device__ __forceinline__ void st8(u32x2 d, rsrc_t rs, unsigned v, unsigned s) { __builtin_amdgcn_raw_buffer_store_b64(d, rs, v, s, AUX_SC1); }
__device__ __forceinline__ void st4(unsigned d, rsrc_t rs, unsigned v, unsigned s) { __builtin_amdgcn_raw_buffer_store_b32(d, rs, v, s, AUX_SC1); }
__device__ __forceinline__ rsrc_t ws_rsrc(unsigned char* ws) { return __builtin_amdgcn_make_buffer_rsrc(ws, 0, WS_USED, 0x00020000); }
__device__ __forceinline__ void glds16(const void* src, LAS unsigned char* dst) {
  __builtin_amdgcn_global_load_lds((const GAS unsigned*)src, (LAS unsigned*)dst, 16, 0, 0);
}
__device__ __forceinline__ u32x4 ws_desc(unsigned char* ws) {
  const unsigned long long a = (unsigned long long)ws;
  return u32x4{(unsigned)a, (unsigned)(a >> 32) & 0xFFFFu, WS_USED, 0x00020000u};
}
#define ALD16(dst, voff, desc, soff) asm volatile("buffer_load_dwordx4 %0, %1, %2, %3 offen sc1" : "=&v"(dst) : "v"(voff), "s"(desc), "s"(soff) : "memory")
#define ALD8(dst, voff, desc, soff) asm volatile("buffer_load_dwordx2 %0, %1, %2, %3 offen sc1" : "=&v"(dst) : "v"(voff), "s"(desc), "s"(soff) : "memory")
#define ALD4(dst, voff, desc, soff) asm volatile("buffer_load_dword %0, %1, %2, %3 offen sc1" : "=&v"(dst) : "v"(voff), "s"(desc), "s"(soff) : "memory")
#define KEEP(x) asm volatile("" ::"v"(x))
#define PINW(w) _Pragma("unroll") for (int k_ = 0; k_ < 7; ++k_) asm volatile("" : "+v"((w)[k_]))
#define BAR() do { asm volatile("s_waitcnt lgkmcnt(0)" ::: "memory"); __builtin_amdgcn_s_barrier(); asm volatile("" ::: "memory"); } while (0)
#define VMWAIT0() asm volatile("s_waitcnt vmcnt(0)" ::: "memory")
#define SCHED() __builtin_amdgcn_sched_barrier(0)
#define TIMING 0
#define LIGHT 0
#define LSTAMP(P, slot) do { } while (0)
#define DBG_DECL
#define DT(slot) do { } while (0)
#define DBG_END(s, last) do { } while (0)
#define DBG_OUT(P, wg, wave) do { } while (0)
#define STAMP(P, slot) do { } while (0)
#define CNT(P, slot, v) do { } while (0)

__device__ __forceinline__ bool any_sent4(u32x4 v) {
  unsigned m = max(max(v.x, v.y), max(v.z, v.w));
  return __builtin_amdgcn_ballot_w64(m == SENT) != 0ull;
}
__device__ __forceinline__ bool any_sent2(u32x2 a, u32x2 b) {
  unsigned m = max(max(a.x, a.y), max(b.x, b.y));
  return __builtin_amdgcn_ballot_w64(m == SENT) != 0ull;
}
__device__ __forceinline__ bool any_sent8(u32x4 a, u32x4 b) {
  unsigned m = max(max(max(a.x, a.y), max(a.z, a.w)), max(max(b.x, b.y), max(b.z, b.w)));
  return __builtin_amdgcn_ballot_w64(m == SENT) != 0ull;
}
__device__ __forceinline__ unsigned mbase(int s, int pol) {
  return OFF_MB + (unsigned)s * 131072u + (((unsigned)pol >> (s >> 4)) & 1u) * 8192u;
}
__device__ __forceinline__ int wait_flags2(rsrc_t rs, unsigned off0, unsigned off1, int need, int& tmo) {
  const int lane = threadIdx.x & 63;
  const unsigned vo = (lane & 1) ? off1 : off0;
  int lo = 0;
  for (unsigned sp = 0;; ++sp) {
    asm volatile("" ::: "memory");
    int v = (int)ld4(rs, vo, 0);
    lo = min(__builtin_amdgcn_readlane(v, 0), __builtin_amdgcn_readlane(v, 1));
    if (lo >= need) break;
    if (sp > SPIN_MAX || tmo) { tmo = 1; lo = NSEG + 1; break; }
    __builtin_amdgcn_s_sleep(4);
  }
  return lo;
}

__global__ void __launch_bounds__(512, 2) prep_kernel(Params P) {
  extern __shared__ __attribute__((aligned(16))) unsigned char smem[];
  LAS unsigned char* lds = (LAS unsigned char*)smem;
  const int tid = threadIdx.x, lane = tid & 63, wave = __builtin_amdgcn_readfirstlane(tid >> 6);
  const int b = blockIdx.x, xcd = b & 7, j = b >> 3;
  const int wh = xcd * 4 + (j >> 3), which = wh >> 4, h = wh & 15, rb = (j >> 2) & 1, cq = j & 3;
  const int n = lane & 15, g = lane >> 4;
  const rsrc_t rs = ws_rsrc(P.ws);
  if (b == 0 && wave == 0) { STAMP(P, 8); LSTAMP(P, 8); }
  if (wave == 0) LSTAMP(P, 2816 + b);

  if (which == 0) {
#pragma unroll
    for (int k = 0; k < 7; ++k) {
      const int fr = wave * 7 + k;
      const int ct = 7 * cq + (fr >> 3), ke = fr & 7;
      const int dp = 32 * (ct >> 1) + 8 * (n >> 2) + 4 * (ct & 1) + (n & 3);
      glds16(P.Wq + ((size_t)(dp * 16 + h) * 256 + 32 * ke + 8 * g), lds + fr * 1024);
    }
  } else {
#pragma unroll
    for (int k = 0; k < 7; ++k) {
      const int i = (wave * 7 + k) * 64 + lane;
      const int e = i / 14, ch = i - e * 14;
      glds16(P.Wo + ((size_t)(h * 256 + e) * 448 + 112 * cq + 8 * ch), lds + (wave * 7 + k) * 1024);
    }
  }
  if (wave == 7) {
    u32x4 xv[8];
    unsigned short wv[7][8];
    unsigned mv[4];
    if (b >= 8) {
#pragma unroll
      for (int k = 0; k < 8; ++k) {
        const int fr = min(b - 8 + 248 * k, 64 * 28 - 1), s = fr / 28, kq = fr - s * 28, q = kq >= 14 ? 1 : 0, k2 = kq - 14 * q;
        xv[k] = *(const u32x4*)(P.x + (size_t)(32 * s + 16 * q + n) * DM + 32 * k2 + 8 * g);
      }
#pragma unroll
      for (int k = 0; k < 7; ++k) {
        const int fr = min(b - 8 + 248 * k, 28 * 56 - 1), cb = fr / 56, u = fr - cb * 56;
#pragma unroll
        for (int jj = 0; jj < 8; ++jj) wv[k][jj] = P.Wd[(size_t)(32 * u + 16 * (jj >> 2) + 4 * g + (jj & 3)) * DM + 16 * cb + n];
      }
    } else if (b < 7) {
#pragma unroll
      for (int k = 0; k < 4; ++k) {
        const int i = k * 64 + lane, gg = (i >> 6) & 3, hi = (i >> 5) & 1, r = (i >> 2) & 7, p = i & 3;
        mv[k] = *(const unsigned*)(P.mem0 + r * DM + 64 * b + 16 * gg + 4 * p + 2 * hi);
      }
    }
    VMWAIT0();
    __syncthreads();
    if (b >= 8) {
#pragma unroll
      for (int k = 0; k < 8; ++k) {
        const int fr = b - 8 + 248 * k;
        if (fr < 64 * 28) st16(xv[k], rs, (unsigned)lane * 16u, OFF_XF + (unsigned)fr * 1024u);
      }
#pragma unroll
      for (int k = 0; k < 7; ++k) {
        const int fr = b - 8 + 248 * k;
        const u32x4 o = {wv[k][0] | ((unsigned)wv[k][1] << 16), wv[k][2] | ((unsigned)wv[k][3] << 16), wv[k][4] | ((unsigned)wv[k][5] << 16), wv[k][6] | ((unsigned)wv[k][7] << 16)};
        if (fr < 28 * 56) st16(o, rs, (unsigned)lane * 16u, OFF_WD + (unsigned)fr * 1024u);
      }
    } else if (b < 7) {
#pragma unroll
      for (int k = 0; k < 4; ++k)
#pragma unroll
        for (int c = 0; c < 4; ++c) st4(san(mv[k]), rs, (unsigned)(k * 64 + lane) * 4u, OFF_MB + K_M + (unsigned)c * 8192u + (unsigned)b * 1024u);
    }
  }
  bf16x8 a[2][8];
  const int kk = 7 * rb + (wave < 7 ? wave : 0);
  if (wave < 7) {
    const unsigned short* W1 = which ? P.Wv : P.Wk;
#pragma unroll
    for (int T = 0; T < 2; ++T) {
      const int d = 32 * kk + 8 * (n >> 2) + 4 * T + (n & 3);
      const unsigned short* p = W1 + ((size_t)(d * 16 + h) * 256 + 8 * g);
#pragma unroll
      for (int ke = 0; ke < 8; ++ke) a[T][ke] = *(const bf16x8*)(p + 32 * ke);
    }
    VMWAIT0();
    __syncthreads();
  }
  if (b == 0 && wave == 0) STAMP(P, 10);
  if (wave == 0) LSTAMP(P, 2560 + b);
  {
    const u32x4 sv = {SENT, SENT, SENT, SENT};
#pragma unroll
    for (int k = 0; k < 4; ++k) {
      const unsigned o = (unsigned)b * 32768u + (unsigned)(k * 512 + tid) * 16u;
      if (!(o >= K_M && o < K_M + 32768u)) st16(sv, rs, OFF_MB + o, 0);
    }
    if (b == 0 && tid == 0) *(unsigned*)(P.ws + FL_ROT) = (unsigned)__builtin_amdgcn_s_getreg(20 | (3 << 11)) & 7u;
    if (b == 3 && tid < (int)FL_COUNT) st4(tid == (int)FL_COUNT - 1 ? SENT : 0u, rs, OFF_FL + (unsigned)tid * FS, 0);
  }
  if (wave == 7) { LSTAMP(P, 2048 + b); return; }
  if (b == 0 && wave == 0) STAMP(P, 11);
  f32x4 acc[2][7];
#pragma unroll
  for (int c = 0; c < 7; ++c) { acc[0][c] = f32x4{0, 0, 0, 0}; acc[1][c] = f32x4{0, 0, 0, 0}; }
  if (which == 0) {
#pragma unroll
    for (int ke = 0; ke < 8; ++ke)
#pragma unroll
      for (int c = 0; c < 7; ++c) {
        const bf16x8 bb = *(const LAS bf16x8*)(lds + (c * 8 + ke) * 1024 + lane * 16);
        acc[0][c] = mfma32(a[0][ke], bb, acc[0][c]);
        acc[1][c] = mfma32(a[1][ke], bb, acc[1][c]);
      }
  } else {
    const LAS unsigned char* tb = lds + (8 * g + (n >> 2)) * 224 + (n & 3) * 8;
#pragma unroll
    for (int ke = 0; ke < 8; ++ke)
#pragma unroll
      for (int c = 0; c < 7; ++c) {
        typedef short v4s __attribute__((ext_vector_type(4)));
        const v4s lo = __builtin_amdgcn_ds_read_tr16_b64_v4i16((LAS v4s*)(tb + ke * 32 * 224 + c * 32));
        const v4s hi = __builtin_amdgcn_ds_read_tr16_b64_v4i16((LAS v4s*)(tb + ke * 32 * 224 + 4 * 224 + c * 32));
        const bf16x8 bb = __builtin_bit_cast(bf16x8, __builtin_shufflevector(lo, hi, 0, 1, 2, 3, 4, 5, 6, 7));
        acc[0][c] = mfma32(a[0][ke], bb, acc[0][c]);
        acc[1][c] = mfma32(a[1][ke], bb, acc[1][c]);
      }
  }
  const float sc = which ? 1.0f : 0.0625f * 1.4426950408889634f;
  const unsigned dst = (which ? OFF_WV : OFF_WK) + (unsigned)((h * 28 + 7 * cq) * 14 + kk) * 1024u;
#pragma unroll
  for (int c = 0; c < 7; ++c) {
    const f32x4 u = acc[0][c] * sc, v = acc[1][c] * sc;
    const u32x4 o = {pk(u[0], u[1]), pk(u[2], u[3]), pk(v[0], v[1]), pk(v[2], v[3])};
    st16(o, rs, (unsigned)lane * 16u, dst + (unsigned)c * 14u * 1024u);
  }
  if (b == 0 && wave == 0) { STAMP(P, 9); LSTAMP(P, 9); }
  if (wave == 0) LSTAMP(P, 2304 + b);
}

__device__ __forceinline__ u32x4 gather_m(const int* td_, u32x4 dsc, unsigned voff, unsigned so0, LAS unsigned char* dst, bool act, int& tmo, unsigned& nfail) {
  u32x4 va, vb;
  ALD16(va, voff, dsc, so0);
  const unsigned so1 = so0 + K_REP;
  if (OVERLAP) {
    DELAY(D_GAP, 4);
    ALD16(vb, voff, dsc, so1);
    asm volatile("s_waitcnt vmcnt(1)" : "+v"(va)::"memory");
  } else {
    vb = u32x4{0, 0, 0, 0};
    asm volatile("s_waitcnt vmcnt(0)" : "+v"(va)::"memory");
  }
  if (__builtin_expect(any_sent4(va), 0)) {
    if (TIMING) ++nfail;
    if (!OVERLAP) ALD16(vb, voff, dsc, so1);
    asm volatile("s_waitcnt vmcnt(0)" : "+v"(vb)::"memory");
    va = vb;
    for (unsigned sp = 2; any_sent4(va); ++sp) {
      asm volatile("" ::: "memory");
      const unsigned r = sp % 3u, so = r == 2u ? (so0 ^ 8192u) : so0 + r * K_REP;
      ALD16(va, voff, dsc, so);
      asm volatile("s_waitcnt vmcnt(0)" : "+v"(va)::"memory");
      if (sp > SPIN_MAX || tmo) { tmo = 1; break; }
    }
  }
  if (act) *(LAS u32x4*)dst = va;
  return vb;
}

__device__ __forceinline__ void probe_pol(const Params& P) {
  if (threadIdx.x >= 64) return;
  const rsrc_t rs = ws_rsrc(P.ws);
  __builtin_amdgcn_s_sleep(64);
  unsigned pol = 0;
  for (int round = 0; round < 3; ++round) {
    int dsum = 0;
#pragma unroll
    for (int p = 0; p < 4; ++p) {
      unsigned t[2];
#pragma unroll
      for (int cls = 0; cls < 2; ++cls) {
        const unsigned off = OFF_MB + (unsigned)(p * 16 + round) * 131072u + K_PROBE + (unsigned)cls * 8192u;
        const unsigned long long c0 = __builtin_amdgcn_s_memtime();
        unsigned v = ld4(rs, 0, off);
        asm volatile("s_waitcnt vmcnt(0)" : "+v"(v)::"memory");
        const unsigned long long c1 = __builtin_amdgcn_s_memtime();
        t[cls] = (unsigned)(c1 - c0);
      }
      const int d = max(-300, min(300, (int)t[0] - (int)t[1]));
      dsum += (p & 1) ? -d : d;
    }
    pol = 0;
#pragma unroll
    for (int p = 0; p < 4; ++p) pol |= (((dsum > 0) ? 1u : 0u) ^ (unsigned)(p & 1)) << p;
    if (dsum > 450 || dsum < -450) break;
  }
  if (threadIdx.x == 0) {
    st4(pol, rs, FL_POL, 0);
  }
}
__device__ __forceinline__ int get_pol(rsrc_t rs, int& tmo) {
  unsigned v;
  for (unsigned sp = 0;; ++sp) {
    asm volatile("" ::: "memory");
    v = (unsigned)__builtin_amdgcn_readfirstlane((int)ld4(rs, 0, FL_POL));
    if (v != SENT) break;
    if (sp > SPIN_MAX) { tmo = 1; v = 0; break; }
    __builtin_amdgcn_s_sleep(4);
  }
  return (int)v;
}

__device__ __forceinline__ void l2_help(const Params& P, unsigned base, unsigned stride, int nfr) {
  const int lane = threadIdx.x & 63, wave = threadIdx.x >> 6;
  u32x4 acc = {0, 0, 0, 0};
  for (int f0 = wave * 12; f0 < nfr; f0 += 96) {
    u32x4 v[12];
#pragma unroll
    for (int k = 0; k < 12; ++k) {
      const int f = min(f0 + k, nfr - 1), t = f / 6;
      v[k] = *(const u32x4*)(P.ws + base + (size_t)t * stride + (size_t)(8 + f - 6 * t) * 1024 + lane * 16);
    }
#pragma unroll
    for (int k = 0; k < 12; ++k) acc ^= v[k];
  }
  asm volatile("" ::"v"(acc));
}

__device__ __forceinline__ void score_main(const Params& P, int idx, int wave, LAS unsigned char* lds) {
  const int lane = threadIdx.x & 63;
  const int h = idx >> 1, q = idx & 1;
  const rsrc_t rs = ws_rsrc(P.ws);
  LAS unsigned char* img = lds;
  LAS float* red = (LAS float*)(lds + 8192);
  const int g = lane >> 4, n = lane & 15;
  const int idxv = (n & 8) ? 0xEEEE : 0x4444;
  s16x4 E;
#pragma unroll
  for (int jj = 0; jj < 4; ++jj) E[jj] = (((4 * g + jj) & 7) == n) ? (short)0x3F80 : (short)0;
  int tmo = 0;
  unsigned nfail = 0;
  if (idx == 0 && wave == 0) STAMP(P, 0);
  bf16x16 w0[7], w1[7];
  const int Pp = 7 * q + wave;
  {
    const unsigned char* b0 = P.ws + OFF_WK + ((size_t)(h * 28 + 2 * Pp) * 14) * 1024 + lane * 16;
#pragma unroll
    for (int ks = 0; ks < 7; ++ks) {
      w0[ks] = cat16(*(const bf16x8*)(b0 + (2 * ks) * 1024), *(const bf16x8*)(b0 + (2 * ks + 1) * 1024));
      w1[ks] = cat16(*(const bf16x8*)(b0 + (14 + 2 * ks) * 1024), *(const bf16x8*)(b0 + (15 + 2 * ks) * 1024));
    }
  }
  const int pol = get_pol(rs, tmo) ^ POLX;
  const unsigned gloc = (unsigned)(wave * 56 + (lane < 56 ? lane : 55)) * 16u;
  const unsigned gvoff = gloc + K_M;
  LAS unsigned char* gdst = img + gloc;
  const LAS unsigned char* afp = img + lane * 16;
  const LAS unsigned char* mp = img + (Pp >> 1) * 1024 + (2 * (Pp & 1) + (g >> 1)) * 256 + (n & 7) * 16 + (g & 1) * 8;
  const unsigned mpa = (unsigned)(size_t)mp;
  LAS float* redw = red + wave * 64 + n * 8 + 4 * g;
  const unsigned xfv = OFF_XF + (unsigned)Pp * 1024u + lane * 16, xfv1 = xfv + 14 * 1024;
  LAS float* red2 = (LAS float*)(lds + 12288);
  LAS float* red2w = red2 + wave * 256 + (g & 1) * 64 + n * 4;
  const LAS float* red2r = red2 + lane * 4;
  const unsigned sxmv = OFF_KM + (unsigned)idx * 1024u + lane * 16;
  const unsigned smv = K_SM + (unsigned)idx * 256u + lane * 4u;
  PINW(w0); PINW(w1);
  const u32x4 dsc = ws_desc(P.ws);
  u32x4 xf0, xf1;
  { const unsigned so = 0; ALD16(xf0, xfv, dsc, so); ALD16(xf1, xfv1, dsc, so); }
  TUNE_DECL
  DBG_DECL
  for (int s = 0; s < NSEG; ++s) {
    const unsigned mb = mbase(s, pol);
    if (s > 0) DELAY(D_MS, 0);
    DT(0);
    const u32x4 pend = gather_m(TDP, dsc, gvoff, mb, gdst, lane < 56, tmo, nfail);
    DT(1);
    BAR();
    DT(2);
    f32x4 c0, c1;
    u32x4 mf;
    {
      u32x2 mlo, mhi;
      asm volatile("ds_read2_b32 %0, %2 offset0:0 offset1:32\n\tds_read2_b32 %1, %2 offset0:1 offset1:33" : "=&v"(mlo), "=&v"(mhi) : "v"(mpa) : "memory");
      bf16x8 a[7];
#pragma unroll
      for (int ks = 0; ks < 7; ++ks) a[ks] = *(const LAS bf16x8*)(afp + ks * 1024);
      SCHED();
      c0 = f32x4{0, 0, 0, 0}; c1 = f32x4{0, 0, 0, 0};
#pragma unroll
      for (int ks = 0; ks < 7; ++ks) {
        c0 = smf(a[ks], w0[ks], c0, idxv);
        c1 = smf(a[ks], w1[ks], c1, idxv);
      }
      SCHED();
      asm volatile("s_waitcnt lgkmcnt(0)" : "+v"(mlo), "+v"(mhi)::"memory");
      mf = u32x4{mlo.x, mlo.y, mhi.x, mhi.y};
    }
    SCHED();
    const u32x2 t0 = {pk(c0[0], c0[1]), pk(c0[2], c0[3])}, t1 = {pk(c1[0], c1[1]), pk(c1[2], c1[3])};
    const f32x4 z = {0, 0, 0, 0};
    SCHED();
    const f32x4 y0 = mfma16(as_s4(t0), E, z), y1 = mfma16(as_s4(t1), E, z);
    SCHED();
    const u32x4 kf = {pk(y0[0], y0[1]), pk(y0[2], y0[3]), pk(y1[0], y1[1]), pk(y1[2], y1[3])};
    const f32x4 S = mfma32(as_b8(kf), as_b8(mf), z);
    if (n < 8 && g < 2) *(LAS f32x4*)redw = S;
    VMWAIT0();
    KEEP(pend);
    DT(3);
    BAR();
    DT(4);
    if (wave == 0 && s < NSEG - 1) {
      const float v0 = red[lane], v1 = red[64 + lane], v2 = red[128 + lane], v3 = red[192 + lane], v4 = red[256 + lane], v5 = red[320 + lane], v6 = red[384 + lane];
      const float v = ((v0 + v1) + (v2 + v3)) + ((v4 + v5) + v6);
      const unsigned o = san(__builtin_bit_cast(unsigned, v));
      st4(o, rs, smv, mb);
      st4(o, rs, smv + K_REP, mb);
      st4(o, rs, smv, mb ^ 8192u);
    }
    DT(5);
    if (idx == 0 && wave == 2 && (s & 7) == 0) LSTAMP(P, 64 + s);
    DBG_END(s, 5);
    asm volatile("" : "+v"(xf0), "+v"(xf1));
    {
      const f32x4 d0 = mfma32(as_b8(kf), as_b8(xf0), z), d1 = mfma32(as_b8(kf), as_b8(xf1), z);
      if (g < 2) { *(LAS f32x4*)red2w = d0; *(LAS f32x4*)(red2w + 128) = d1; }
    }
    BAR();
    if (wave == 1) {
      f32x4 v = *(const LAS f32x4*)red2r;
#pragma unroll
      for (int w = 1; w < 7; ++w) v += *(const LAS f32x4*)(red2r + w * 256);
      st16(__builtin_bit_cast(u32x4, v), rs, sxmv, (unsigned)s * KM_S);
    }
    if (s < NSEG - 1) DELAY(D_BULK, 5);
    { const unsigned so = (unsigned)min(s + 1, NSEG - 1) * XF_S; ALD16(xf0, xfv, dsc, so); ALD16(xf1, xfv1, dsc, so); }
  }
  DBG_OUT(P, idx, wave);
  VMWAIT0();
  BAR();
  CNT(P, 0, nfail);
  if (idx == 0 && wave == 0) { STAMP(P, 1); LSTAMP(P, 1); }
}

__device__ __forceinline__ void score_sx(const Params& P, int idx, LAS unsigned char* lds) {
  const int lane = threadIdx.x & 63;
  const int wave = 7;
  const int h = idx >> 1, q = idx & 1;
  const rsrc_t rs = ws_rsrc(P.ws);
  LAS unsigned char* img = lds;
  const int g = lane >> 4, n = lane & 15;
  const int idxv = (n & 8) ? 0xEEEE : 0x4444;
  s16x4 E;
#pragma unroll
  for (int jj = 0; jj < 4; ++jj) E[jj] = (((4 * g + jj) & 7) == n) ? (short)0x3F80 : (short)0;
  int tmo = 0;
  unsigned nfail = 0;
  const int pol = get_pol(rs, tmo) ^ POLX;
  if (idx == 0) STAMP(P, 2);
  const unsigned f0 = FL_T1K + (unsigned)(h * 2) * FS, f1 = f0 + FS;
  int known = wait_flags2(rs, f0, f1, 1, tmo);
  if (idx == 0) STAMP(P, 3);
  int fl = 0;
  const unsigned flv = (lane & 1) ? f1 : f0;
  u32x4 kx[14];
  const unsigned kxv = OFF_KX + (unsigned)((h * 2 + q) * 14) * 1024u + lane * 16;
  const u32x4 dsc = ws_desc(P.ws);
  const unsigned kxa[4] = {kxv, kxv + 4096, kxv + 8192, kxv + 12288};
#define KXLD(k, so) asm volatile("buffer_load_dwordx4 %0, %1, %2, %3 offen offset:%c4 sc1" : "=&v"(kx[k]) : "v"(kxa[(k) >> 2]), "s"(dsc), "s"(so), "i"(((k) & 3) * 1024) : "memory")
  {
    const unsigned so = 0;
    KXLD(0, so); KXLD(1, so); KXLD(2, so); KXLD(3, so); KXLD(4, so); KXLD(5, so); KXLD(6, so);
    KXLD(7, so); KXLD(8, so); KXLD(9, so); KXLD(10, so); KXLD(11, so); KXLD(12, so); KXLD(13, so);
  }
  const unsigned gloc = (unsigned)(wave * 56 + (lane < 56 ? lane : 55)) * 16u;
  const unsigned gvoff = gloc + K_M;
  LAS unsigned char* gdst = img + gloc;
  const LAS unsigned char* afp = img + lane * 16;
  const unsigned sxv = K_SX + (unsigned)idx * 256u + (unsigned)(g * 8 + (n & 7)) * 8u;
  const unsigned chf = FL_CH + (unsigned)idx * FS;
  __builtin_amdgcn_s_setprio(3);
  TUNE_DECL
  for (int s = 0; s < NSEG; ++s) {
    const unsigned mb = mbase(s, pol);
    if (s > 0) DELAY(D_MS, 0);
    const u32x4 pend = gather_m(TDP, dsc, gvoff, mb, gdst, lane < 56, tmo, nfail);
    BAR();
    {
      bf16x8 a[7];
#pragma unroll
      for (int ks = 0; ks < 7; ++ks) a[ks] = *(const LAS bf16x8*)(afp + ks * 1024);
      SCHED();
      f32x4 c0 = {0, 0, 0, 0};
#pragma unroll
      for (int ks = 0; ks < 7; ++ks) c0 = smf(a[ks], cat16(as_b8(kx[2 * ks]), as_b8(kx[2 * ks + 1])), c0, idxv);
      VMWAIT0();
      KEEP(pend);
      const u32x2 t0 = {pk(c0[0], c0[1]), pk(c0[2], c0[3])};
      const f32x4 z = {0, 0, 0, 0};
      const f32x4 y0 = mfma16(as_s4(t0), E, z);
      const u32x2 o = {san(pk(y0[0], y0[1])), san(pk(y0[2], y0[3]))};
      if (n < 8 && s < NSEG - 1) {
        st8(o, rs, sxv, mb);
        st8(o, rs, sxv + K_REP, mb);
        st8(o, rs, sxv, mb ^ 8192u);
      }
      if (lane == 0) st4((unsigned)s, rs, chf, 0);
      SCHED();
    }
    BAR();
    BAR();
    if (s < NSEG - 1) DELAY(D_BULK, 5);
    const int sn = min(s + 1, NSEG - 2);
    asm volatile("" : "+v"(fl));
    known = max(known, min(__builtin_amdgcn_readlane(fl, 0), __builtin_amdgcn_readlane(fl, 1)));
    if (__builtin_expect(known <= sn, 0)) { known = wait_flags2(rs, f0, f1, sn + 1, tmo); CNT(P, 4, 1); }
    { const unsigned z = 0; ALD4(fl, flv, dsc, z); }
    {
      const unsigned so = (unsigned)sn * KX_S;
      KXLD(0, so); KXLD(1, so); KXLD(2, so); KXLD(3, so); KXLD(4, so); KXLD(5, so); KXLD(6, so);
      KXLD(7, so); KXLD(8, so); KXLD(9, so); KXLD(10, so); KXLD(11, so); KXLD(12, so); KXLD(13, so);
    }
    SCHED();
  }
  VMWAIT0();
  BAR();
  if (lane == 0) st4((unsigned)NSEG, rs, chf, 0);
  CNT(P, 0, nfail);
}

__device__ __forceinline__ void value_wg(const Params& P, int cb, LAS unsigned char* lds) {
  const int tid = threadIdx.x, lane = tid & 63, wave = __builtin_amdgcn_readfirstlane(tid >> 6);
  const rsrc_t rs = ws_rsrc(P.ws);
  LAS unsigned char* img = lds;
  LAS float* red = (LAS float*)(lds + 8192);
  const int g = lane >> 4, n = lane & 15, sel = n >> 3, i = n & 7;
  const int idxv = (n & 8) ? 0xEEEE : 0x4444;
  const int h0 = 2 * wave, hs = h0 + sel;
  int tmo = 0;
  unsigned nfail = 0, nfx = 0, nfm = 0;
  bf16x16 w0[7], w1[7];
  {
    const unsigned char* b0 = P.ws + OFF_WV + ((size_t)(h0 * 28 + cb) * 14) * 1024 + lane * 16;
    const unsigned char* b1 = b0 + (size_t)28 * 14 * 1024;
#pragma unroll
    for (int ks = 0; ks < 7; ++ks) {
      w0[ks] = cat16(*(const bf16x8*)(b0 + (2 * ks) * 1024), *(const bf16x8*)(b0 + (2 * ks + 1) * 1024));
      w1[ks] = cat16(*(const bf16x8*)(b1 + (2 * ks) * 1024), *(const bf16x8*)(b1 + (2 * ks + 1) * 1024));
    }
  }
  const int pol = get_pol(rs, tmo) ^ POLX;
  const unsigned fa = FL_T1V + (unsigned)(h0 * 2 + (cb >= 14 ? 1 : 0)) * FS, fb = fa + 2 * FS;
  int known = 0;
  const unsigned vxv = OFF_VX + (unsigned)(h0 * 28 + cb) * 1024u + lane * 16, vxv1 = vxv + 28 * 1024;
  const unsigned flv = (lane & 1) ? fb : fa;
  const unsigned gloc = (unsigned)(wave * 56 + (lane < 56 ? lane : 55)) * 16u;
  const unsigned gvoff = gloc + K_M;
  LAS unsigned char* gdst = img + gloc;
  const LAS unsigned char* afp = img + lane * 16;
  const unsigned vmv = OFF_VM + (unsigned)(h0 * 28 + cb) * 512u + lane * 8;
  const unsigned sxv = K_SX + (unsigned)(hs * 2) * 256u + (unsigned)(g * 8 + i) * 8u;
  const unsigned smv = K_SM + (unsigned)(hs * 2) * 256u + (unsigned)(i * 8 + 4 * (g & 1)) * 4u;
  LAS float* redw = red + wave * 128 + i * 16 + 4 * g;
  const LAS float* redr = red + ((lane >> 2) & 7) * 16 + 4 * (lane & 3) + 2 * (lane >> 5);
  const unsigned mv = K_M + (unsigned)cb * 256u + lane * 4u;
  const unsigned chf = FL_CH + (unsigned)(32 + cb) * FS;
  u32x2 vm0 = {0, 0}, vm1 = {0, 0};
  PINW(w0); PINW(w1);
  const u32x4 dsc = ws_desc(P.ws);
  TUNE_DECL
  DBG_DECL
  for (int s = 0; s < NSEG - 1; ++s) {
    const unsigned mb = mbase(s, pol), mbn = mbase(s + 1, pol);
    if (s > 0) DELAY(D_MV, 1);
    DT(0);
    const u32x4 pend = gather_m(TDP, dsc, gvoff, mb, gdst, lane < 56, tmo, nfail);
    DT(1);
    BAR();
    if (__builtin_expect(known <= s, 0)) { known = wait_flags2(rs, fa, fb, s + 1, tmo); CNT(P, 5, 1); }
    bf16x8 a[7];
#pragma unroll
    for (int ks = 0; ks < 7; ++ks) a[ks] = *(const LAS bf16x8*)(afp + ks * 1024);
    u32x4 vx0, vx1;
    {
      const unsigned so = (unsigned)s * VX_S;
      ALD16(vx0, vxv, dsc, so);
      ALD16(vx1, vxv1, dsc, so);
    }
    st8(vm0, rs, vmv, (unsigned)max(s - 1, 0) * VM_S);
    st8(vm1, rs, vmv + 28 * 512, (unsigned)max(s - 1, 0) * VM_S);
    if (wave == 1 && lane == 0) st4((unsigned)max(s - 1, 0), rs, chf, 0);
    int fl;
    { const unsigned z = 0; ALD4(fl, flv, dsc, z); }
    SCHED();
    DELAY(D_SX, 2);
    u32x2 x0, x1, y0, y1;
    u32x4 ua, ub, wa, wb;
    const unsigned sxv1 = sxv + 256, smv1 = smv + 256, mb1 = mb + K_REP;
    ALD8(x0, sxv, dsc, mb);
    ALD8(x1, sxv1, dsc, mb);
    SCHED();
    f32x4 c0 = {0, 0, 0, 0}, c1 = {0, 0, 0, 0};
#pragma unroll
    for (int ks = 0; ks < 7; ++ks) {
      c0 = smf(a[ks], w0[ks], c0, idxv);
      c1 = smf(a[ks], w1[ks], c1, idxv);
    }
    vm0 = u32x2{pk(c0[0], c0[1]), pk(c0[2], c0[3])};
    vm1 = u32x2{pk(c1[0], c1[1]), pk(c1[2], c1[3])};
    SCHED();
    DT(2);
    delay_c<D_G2>();
    ALD8(y0, sxv, dsc, mb1);
    ALD8(y1, sxv1, dsc, mb1);
    DELAY(D_SM, 3);
    ALD16(ua, smv, dsc, mb);
    ALD16(ub, smv1, dsc, mb);
    asm volatile("s_waitcnt vmcnt(4)" : "+v"(x0), "+v"(x1), "+v"(vx0), "+v"(vx1), "+v"(fl)::"memory");
    KEEP(pend);
    known = max(known, min(__builtin_amdgcn_readlane(fl, 0), __builtin_amdgcn_readlane(fl, 1)));
    if (__builtin_expect(any_sent2(x0, x1), 0)) {
      if (TIMING) ++nfx;
      asm volatile("s_waitcnt vmcnt(2)" : "+v"(y0), "+v"(y1)::"memory");
      x0 = y0; x1 = y1;
      for (unsigned sp = 2; any_sent2(x0, x1); ++sp) {
        asm volatile("" ::: "memory");
        const unsigned r = sp % 3u, so = r == 2u ? (mb ^ 8192u) : mb + r * K_REP;
        ALD8(x0, sxv, dsc, so);
        ALD8(x1, sxv1, dsc, so);
        asm volatile("s_waitcnt vmcnt(0)" : "+v"(x0), "+v"(x1)::"memory");
        if (sp > SPIN_MAX || tmo) { tmo = 1; break; }
      }
    }
    DT(3);
    SCHED();
    float t[8] = {bflo(x0.x), bfhi(x0.x), bflo(x0.y), bfhi(x0.y), bflo(x1.x), bfhi(x1.x), bflo(x1.y), bfhi(x1.y)};
    float m1 = vmax(vmax(vmax(t[0], t[1]), vmax(t[2], t[3])), vmax(vmax(t[4], t[5]), vmax(t[6], t[7])));
    m1 = max32(max16(m1));
    float l1 = 0.f;
#pragma unroll
    for (int k = 0; k < 8; ++k) { t[k] = __builtin_amdgcn_exp2f(t[k] - m1); l1 += t[k]; }
    SCHED();
    ALD16(wa, smv, dsc, mb1);
    ALD16(wb, smv1, dsc, mb1);
    SCHED();
    l1 = sum32(sum16(l1));
    const u32x4 pb = {pk(t[0], t[1]), pk(t[2], t[3]), pk(t[4], t[5]), pk(t[6], t[7])};
    const f32x4 z = {0, 0, 0, 0};
    const f32x4 ax0 = mfma32(as_b8(vx0), as_b8(pb), z), ax1 = mfma32(as_b8(vx1), as_b8(pb), z);
    const f32x4 ax = sel ? ax1 : ax0;
    SCHED();
    DT(4);
    asm volatile("s_waitcnt vmcnt(2)" : "+v"(ua), "+v"(ub)::"memory");
    KEEP(y0); KEEP(y1);
    if (__builtin_expect(any_sent8(ua, ub), 0)) {
      if (TIMING) ++nfm;
      asm volatile("s_waitcnt vmcnt(0)" : "+v"(wa), "+v"(wb)::"memory");
      ua = wa; ub = wb;
      for (unsigned sp = 2; any_sent8(ua, ub); ++sp) {
        asm volatile("" ::: "memory");
        const unsigned r = sp % 3u, so = r == 2u ? (mb ^ 8192u) : mb + r * K_REP;
        ALD16(ua, smv, dsc, so);
        ALD16(ub, smv1, dsc, so);
        asm volatile("s_waitcnt vmcnt(0)" : "+v"(ua), "+v"(ub)::"memory");
        if (sp > SPIN_MAX || tmo) { tmo = 1; break; }
      }
    }
    DT(5);
    const f32x4 sm = __builtin_bit_cast(f32x4, ua) + __builtin_bit_cast(f32x4, ub);
    float m2 = vmax(vmax(sm[0], sm[1]), vmax(sm[2], sm[3]));
    m2 = max16(m2);
    const float M = vmax(m1, m2);
    const float al = __builtin_amdgcn_exp2f(m1 - M);
    const float e0 = __builtin_amdgcn_exp2f(sm[0] - M), e1 = __builtin_amdgcn_exp2f(sm[1] - M), e2 = __builtin_amdgcn_exp2f(sm[2] - M), e3 = __builtin_amdgcn_exp2f(sm[3] - M);
    const u32x2 pm = {pk(e0, e1), pk(e2, e3)};
    const f32x4 cx = ax * al;
    const f32x4 r0 = mfma16(as_s4(vm0), as_s4(pm), cx), r1 = mfma16(as_s4(vm1), as_s4(pm), cx);
    const float l2 = sum16((e0 + e1) + (e2 + e3));
    const float rl = __builtin_amdgcn_rcpf(al * l1 + l2);
    f32x4 r = (sel ? r1 : r0) * rl;
#pragma unroll
    for (int k = 0; k < 4; ++k) {
      const float rk = r[k];
      const int o = __builtin_amdgcn_update_dpp(0, __builtin_bit_cast(int, rk), 0x128, 0xf, 0xf, true);
      r[k] += __builtin_bit_cast(float, o);
    }
    if (sel == 0) *(LAS f32x4*)redw = r;
    BAR();
    DT(6);
    if (wave == 0) {
      float a0[8], a1[8];
#pragma unroll
      for (int w = 0; w < 8; ++w) { a0[w] = redr[w * 128]; a1[w] = redr[w * 128 + 1]; }
      const float s0 = ((a0[0] + a0[1]) + (a0[2] + a0[3])) + ((a0[4] + a0[5]) + (a0[6] + a0[7]));
      const float s1 = ((a1[0] + a1[1]) + (a1[2] + a1[3])) + ((a1[4] + a1[5]) + (a1[6] + a1[7]));
      const unsigned o = san(pk(s0, s1));
      st4(o, rs, mv, mbn);
      st4(o, rs, mv + K_REP, mbn);
      st4(o, rs, mv, mbn ^ 8192u);
      asm volatile("s_waitcnt vmcnt(3)" ::: "memory");
    } else {
      VMWAIT0();
    }
    KEEP(wa); KEEP(wb);
    DT(7);
    DBG_END(s, 7);
  }
  DBG_OUT(P, 32 + cb, wave);
  {
    const int s = NSEG - 1;
    const unsigned mb = mbase(s, pol);
    DELAY(D_MV, 1);
    const u32x4 pend = gather_m(TDP, dsc, gvoff, mb, gdst, lane < 56, tmo, nfail);
    BAR();
    st8(vm0, rs, vmv, (unsigned)(s - 1) * VM_S);
    st8(vm1, rs, vmv + 28 * 512, (unsigned)(s - 1) * VM_S);
    f32x4 c0 = {0, 0, 0, 0}, c1 = {0, 0, 0, 0};
#pragma unroll
    for (int ks = 0; ks < 7; ++ks) {
      const bf16x8 a = *(const LAS bf16x8*)(afp + ks * 1024);
      c0 = smf(a, w0[ks], c0, idxv);
      c1 = smf(a, w1[ks], c1, idxv);
    }
    vm0 = u32x2{pk(c0[0], c0[1]), pk(c0[2], c0[3])};
    vm1 = u32x2{pk(c1[0], c1[1]), pk(c1[2], c1[3])};
    st8(vm0, rs, vmv, (unsigned)s * VM_S);
    st8(vm1, rs, vmv + 28 * 512, (unsigned)s * VM_S);
    VMWAIT0();
    KEEP(pend);
    BAR();
    if (wave == 1 && lane == 0) st4((unsigned)NSEG, rs, chf, 0);
    CNT(P, 1, nfail); CNT(P, 2, nfx); CNT(P, 3, nfm);
    if (cb == 0 && wave == 0) STAMP(P, 4);
  }
}

__device__ __forceinline__ void stage_x(const Params& P, int s, LAS unsigned char* buf, int lane, int first, int count) {
  const unsigned char* p = P.ws + OFF_XF + (size_t)s * XF_S + lane * 16;
  for (int k = first; k < first + count; ++k) glds16(p + k * 1024, buf + k * 1024);
}

template <int MODE>
__device__ __forceinline__ void stream_wg(const Params& P, int id, LAS unsigned char* lds) {
  const int tid = threadIdx.x, lane = tid & 63, wave = __builtin_amdgcn_readfirstlane(tid >> 6);
  const rsrc_t rs = ws_rsrc(P.ws);
  const int n = lane & 15, g = lane >> 4;
  bf16x8 w0[14], w1[14];
  if (MODE == 2) {
#pragma unroll 1
    for (int m = 0; m < 2; ++m) {
      const unsigned short* W = m ? P.Wu : P.Wg;
      for (int k = wave; k < 98; k += 8) {
        const int i = k * 64 + lane, d = i / 14, ch = i - d * 14;
        glds16(W + (size_t)d * 1792 + 112 * id + 8 * ch, lds + k * 1024);
      }
      VMWAIT0();
      __syncthreads();
      if (wave < 7) {
        const LAS unsigned char* tb = lds + (8 * g + (n >> 2)) * 224 + (n & 3) * 8 + wave * 32;
#pragma unroll
        for (int kk = 0; kk < 14; ++kk) {
          typedef short v4s __attribute__((ext_vector_type(4)));
          const v4s lo = __builtin_amdgcn_ds_read_tr16_b64_v4i16((LAS v4s*)(tb + kk * 32 * 224));
          const v4s hi = __builtin_amdgcn_ds_read_tr16_b64_v4i16((LAS v4s*)(tb + kk * 32 * 224 + 4 * 224));
          const bf16x8 f = __builtin_bit_cast(bf16x8, __builtin_shufflevector(lo, hi, 0, 1, 2, 3, 4, 5, 6, 7));
          if (m == 0) w0[kk] = f; else w1[kk] = f;
        }
      }
      __syncthreads();
    }
  } else if (wave < 7) {
    const int h = id >> 1, hq = id & 1;
    const unsigned char* b0 = P.ws + (MODE ? OFF_WV : OFF_WK) + ((size_t)(h * 28 + 14 * hq + 2 * wave) * 14) * 1024 + lane * 16;
#pragma unroll
    for (int kk = 0; kk < 14; ++kk) { w0[kk] = *(const bf16x8*)(b0 + kk * 1024); w1[kk] = *(const bf16x8*)(b0 + (14 + kk) * 1024); }
  }
  const unsigned fl = (MODE == 0 ? FL_T1K : MODE == 1 ? FL_T1V : FL_F1) + (unsigned)id * FS;
  if (wave == 7) {
    stage_x(P, 0, lds, lane, 0, 28);
    stage_x(P, 1, lds + 28672, lane, 0, 28);
    asm volatile("s_waitcnt vmcnt(28)" ::: "memory");
    int nb = 2;
    for (int s = 0; s < NSEG; ++s) {
      BAR();
      if (id == 0) STAMP(P, 192 + MODE * 64 + s);
      if (s + 2 < NSEG) { stage_x(P, s + 2, lds + nb * 28672, lane, 0, 28); asm volatile("s_waitcnt vmcnt(28)" ::: "memory"); }
      else VMWAIT0();
      nb = nb == 2 ? 0 : nb + 1;
    }
    BAR();
    if (lane == 0) st4((unsigned)NSEG, rs, fl, 0);
  } else {
    int cbuf = 0;
    for (int s = 0; s < NSEG; ++s) {
      BAR();
      if (wave == 0 && s > 0 && lane == 0) st4((unsigned)(s <= 2 ? s : s - 1), rs, fl, 0);
      const LAS unsigned char* xb = lds + cbuf * 28672 + lane * 16;
      cbuf = cbuf == 2 ? 0 : cbuf + 1;
      f32x4 c00 = {0, 0, 0, 0}, c01 = {0, 0, 0, 0}, c10 = {0, 0, 0, 0}, c11 = {0, 0, 0, 0};
      bf16x8 xa[3], xc[3];
      xa[0] = *(const LAS bf16x8*)(xb); xc[0] = *(const LAS bf16x8*)(xb + 14 * 1024);
      xa[1] = *(const LAS bf16x8*)(xb + 1024); xc[1] = *(const LAS bf16x8*)(xb + 15 * 1024);
#pragma unroll
      for (int kk = 0; kk < 14; ++kk) {
        if (kk + 2 < 14) { xa[(kk + 2) % 3] = *(const LAS bf16x8*)(xb + (kk + 2) * 1024); xc[(kk + 2) % 3] = *(const LAS bf16x8*)(xb + (16 + kk) * 1024); }
        SCHED();
        const bf16x8 x0 = xa[kk % 3], x1 = xc[kk % 3];
        if (MODE == 1) { c00 = mfma32(x0, w0[kk], c00); c01 = mfma32(x0, w1[kk], c01); c10 = mfma32(x1, w0[kk], c10); c11 = mfma32(x1, w1[kk], c11); }
        else { c00 = mfma32(w0[kk], x0, c00); c01 = mfma32(w1[kk], x0, c01); c10 = mfma32(w0[kk], x1, c10); c11 = mfma32(w1[kk], x1, c11); }
        SCHED();
      }
      if (MODE == 2) {
#pragma unroll
        for (int q = 0; q < 2; ++q) {
          const f32x4 cg = q ? c10 : c00, cu = q ? c11 : c01;
          float hv[4];
#pragma unroll
          for (int k = 0; k < 4; ++k) {
            const float gg = cg[k];
            hv[k] = gg * __builtin_amdgcn_rcpf(1.0f + __builtin_amdgcn_exp2f(-1.4426950408889634f * gg)) * cu[k];
          }
          const u32x2 o = {pk(hv[0], hv[1]), pk(hv[2], hv[3])};
          st8(o, rs, OFF_HF + (unsigned)(q * 112 + 7 * id + wave) * 512u + lane * 8, (unsigned)s * HF_S);
        }
      } else if (MODE == 0) {
        const int h = id >> 1, Pp = 7 * (id & 1) + wave;
        const u32x4 o0 = {pk(c00[0], c00[1]), pk(c00[2], c00[3]), pk(c01[0], c01[1]), pk(c01[2], c01[3])};
        const u32x4 o1 = {pk(c10[0], c10[1]), pk(c10[2], c10[3]), pk(c11[0], c11[1]), pk(c11[2], c11[3])};
        st16(o0, rs, OFF_KX + (unsigned)((h * 2 + 0) * 14 + Pp) * 1024u + lane * 16, (unsigned)s * KX_S);
        st16(o1, rs, OFF_KX + (unsigned)((h * 2 + 1) * 14 + Pp) * 1024u + lane * 16, (unsigned)s * KX_S);
      } else {
        const int h = id >> 1, cb = 14 * (id & 1) + 2 * wave;
        const u32x4 o0 = {pk(c00[0], c00[1]), pk(c00[2], c00[3]), pk(c10[0], c10[1]), pk(c10[2], c10[3])};
        const u32x4 o1 = {pk(c01[0], c01[1]), pk(c01[2], c01[3]), pk(c11[0], c11[1]), pk(c11[2], c11[3])};
        st16(o0, rs, OFF_VX + (unsigned)(h * 28 + cb) * 1024u + lane * 16, (unsigned)s * VX_S);
        st16(o1, rs, OFF_VX + (unsigned)(h * 28 + cb + 1) * 1024u + lane * 16, (unsigned)s * VX_S);
      }
      if (s < 2) VMWAIT0(); else asm volatile("s_waitcnt vmcnt(2)" ::: "memory");
    }
    VMWAIT0();
    BAR();
  }
}

template <int SLEEP>
__device__ __forceinline__ void wait_lanes(rsrc_t rs, unsigned voff, int need, int& tmo) {
  for (unsigned sp = 0;; ++sp) {
    asm volatile("" ::: "memory");
    const int v = (int)ld4(rs, voff, 0);
    if (__builtin_amdgcn_ballot_w64(v < need) == 0ull) break;
    if (sp > SPIN_MAX || tmo) { tmo = 1; break; }
    __builtin_amdgcn_s_sleep(SLEEP);
  }
}

__device__ __forceinline__ void task_sxx(const Params& P, int s, LAS unsigned char* lds, int& tmo) {
  int tid = threadIdx.x;
  asm volatile("" : "+v"(tid));
  const int lane = tid & 63, wave = __builtin_amdgcn_readfirstlane(tid >> 6);
  const rsrc_t rs = ws_rsrc(P.ws);
  __syncthreads();
  stage_x(P, s, lds, lane, wave * 4, wave < 7 ? 4 : 0);
  if (wave == 0) wait_lanes<24>(rs, FL_T1K + (unsigned)(lane & 31) * FS, s + 1, tmo);
  BAR();
  const unsigned kv = lane * 16;
  const unsigned so = OFF_KX + (unsigned)(2 * wave * 2 * 14) * 1024u + (unsigned)s * KX_S;
  u32x4 ka[2][7], kb[2][7];
#pragma unroll
  for (int k = 0; k < 7; ++k) { ka[0][k] = ld16(rs, kv, so + k * 1024); kb[0][k] = ld16(rs, kv, so + (14 + k) * 1024); }
  asm volatile("s_waitcnt vmcnt(14)" ::: "memory");
  __syncthreads();
  const LAS unsigned char* xb = lds + lane * 16;
  f32x4 c00 = {0, 0, 0, 0}, c01 = {0, 0, 0, 0}, c11 = {0, 0, 0, 0};
#pragma unroll
  for (int c = 0; c < 4; ++c) {
    if (c + 1 < 4) {
      const int hn = (c + 1) >> 1, kn = ((c + 1) & 1) * 7;
#pragma unroll
      for (int k = 0; k < 7; ++k) { ka[(c + 1) & 1][k] = ld16(rs, kv, so + (hn * 28 + kn + k) * 1024); kb[(c + 1) & 1][k] = ld16(rs, kv, so + (hn * 28 + 14 + kn + k) * 1024); }
    }
    SCHED();
#pragma unroll
    for (int k = 0; k < 7; ++k) {
      const int kk = (c & 1) * 7 + k;
      const bf16x8 x0 = *(const LAS bf16x8*)(xb + kk * 1024), x1 = *(const LAS bf16x8*)(xb + (14 + kk) * 1024);
      c00 = mfma32(as_b8(ka[c & 1][k]), x0, c00);
      c01 = mfma32(as_b8(ka[c & 1][k]), x1, c01);
      c11 = mfma32(as_b8(kb[c & 1][k]), x1, c11);
    }
    if (c & 1) {
      const unsigned o = OFF_SXX + (unsigned)((2 * wave + (c >> 1)) * 3) * 1024u + (unsigned)s * SXX_S;
      st16(__builtin_bit_cast(u32x4, c00), rs, kv, o);
      st16(__builtin_bit_cast(u32x4, c01), rs, kv, o + 1024);
      st16(__builtin_bit_cast(u32x4, c11), rs, kv, o + 2048);
      c00 = f32x4{0, 0, 0, 0}; c01 = f32x4{0, 0, 0, 0}; c11 = f32x4{0, 0, 0, 0};
    }
    SCHED();
  }
  VMWAIT0();
  __syncthreads();
  if (tid == 0) st4(1u, rs, FL_T2 + (unsigned)s * FS, 0);
}

__device__ __forceinline__ void task_ffn2(const Params& P, int sp, LAS unsigned char* lds, int& tmo) {
  int tid = threadIdx.x;
  asm volatile("" : "+v"(tid));
  const int lane = tid & 63, wave = __builtin_amdgcn_readfirstlane(tid >> 6);
  const rsrc_t rs = ws_rsrc(P.ws);
  __syncthreads();
  const unsigned char* wd = P.ws + OFF_WD + (size_t)(4 * (wave < 7 ? wave : 0)) * 56 * 1024 + lane * 16;
  bf16x8 wf[4][4];
#pragma unroll
  for (int uu = 0; uu < 4; ++uu)
#pragma unroll
    for (int c = 0; c < 4; ++c) wf[uu][c] = *(const bf16x8*)(wd + (size_t)(c * 56 + uu) * 1024);
  if (wave == 0) wait_lanes<24>(rs, FL_F1 + (unsigned)(lane & 15) * FS, 2 * sp + 2, tmo);
  BAR();
  f32x4 acc[4][4];
#pragma unroll
  for (int c = 0; c < 4; ++c)
#pragma unroll
    for (int q = 0; q < 4; ++q) acc[c][q] = f32x4{0, 0, 0, 0};
#pragma unroll 1
  for (int half = 0; half < 2; ++half) {
    if (half) __syncthreads();
    {
      u32x4 v[14];
#pragma unroll
      for (int k = 0; k < 14; ++k) {
        const int pc = wave * 14 + k, tile = pc / 28, f2 = pc - tile * 28;
        v[k] = ld16(rs, lane * 16, OFF_HF + (unsigned)(2 * sp + (tile >> 1)) * HF_S + (unsigned)((tile & 1) * 112 + 56 * half + 2 * f2) * 512u);
      }
#pragma unroll
      for (int k = 0; k < 14; ++k) *(LAS u32x4*)(lds + (wave * 14 + k) * 1024 + lane * 16) = v[k];
    }
    __syncthreads();
    if (wave < 7) {
      const LAS unsigned char* hb = lds + lane * 8;
#pragma unroll 1
      for (int u0 = 0; u0 < 28; u0 += 4) {
        const int ug = 28 * half + u0;
        const int un = ug + 4 < 56 ? ug + 4 : ug;
#pragma unroll
        for (int uu = 0; uu < 4; ++uu) {
          const int u = u0 + uu;
          u32x4 hf[4];
#pragma unroll
          for (int q = 0; q < 4; ++q) {
            const u32x2 lo = *(const LAS u32x2*)(hb + (q * 56 + 2 * u) * 512), hi = *(const LAS u32x2*)(hb + (q * 56 + 2 * u + 1) * 512);
            hf[q] = u32x4{lo.x, lo.y, hi.x, hi.y};
          }
#pragma unroll
          for (int q = 0; q < 4; ++q)
#pragma unroll
            for (int c = 0; c < 4; ++c) acc[c][q] = mfma32(wf[uu][c], as_b8(hf[q]), acc[c][q]);
          SCHED();
#pragma unroll
          for (int c = 0; c < 4; ++c) wf[uu][c] = *(const bf16x8*)(wd + (size_t)(c * 56 + un + uu) * 1024);
          SCHED();
        }
      }
    } else if (half == 0) {
      const unsigned short* xp = P.x + (size_t)(64 * sp + lane) * DM;
      float acc1 = 0.f;
#pragma unroll 7
      for (int k = 0; k < 56; ++k) {
        const u32x4 xv = *(const u32x4*)(xp + 8 * k), gv = *(const u32x4*)(P.gw + 8 * k);
        acc1 += bflo(xv.x) * bflo(gv.x) + bfhi(xv.x) * bfhi(gv.x) + bflo(xv.y) * bflo(gv.y) + bfhi(xv.y) * bfhi(gv.y) +
                bflo(xv.z) * bflo(gv.z) + bfhi(xv.z) * bfhi(gv.z) + bflo(xv.w) * bflo(gv.w) + bfhi(xv.w) * bfhi(gv.w);
      }
      const float gate = __builtin_amdgcn_rcpf(1.0f + __builtin_amdgcn_exp2f(-1.4426950408889634f * acc1));
      st4(__builtin_bit_cast(unsigned, gate), rs, (unsigned)lane * 4u, OFF_G + (unsigned)(64 * sp) * 4u);
    }
  }
  if (wave < 7) {
#pragma unroll
    for (int c = 0; c < 4; ++c)
#pragma unroll
      for (int q = 0; q < 4; ++q) {
        const u32x2 o = {pk(acc[c][q][0], acc[c][q][1]), pk(acc[c][q][2], acc[c][q][3])};
        st8(o, rs, lane * 8, OFF_BF + (unsigned)((q & 1) * 28 + 4 * wave + c) * 512u + (unsigned)(2 * sp + (q >> 1)) * BF_S);
      }
  }
  VMWAIT0();
  __syncthreads();
  if (tid == 0) st4(1u, rs, FL_F2 + (unsigned)sp * FS, 0);
}

template <int Q>
__device__ __forceinline__ void attn_softmax(const f32x4 sm, const f32x4 ta, const f32x4 tb, int g, int n, u32x4& pt, u32x2& pm) {
  const float NEG = -3.0e38f;
  float t[8];
#pragma unroll
  for (int k = 0; k < 4; ++k) {
    const bool causal = (4 * g + k) <= n;
    t[k] = Q ? ta[k] : (causal ? ta[k] : NEG);
    t[4 + k] = Q ? (causal ? tb[k] : NEG) : NEG;
  }
  float m = vmax(vmax(vmax(sm[0], sm[1]), vmax(sm[2], sm[3])), vmax(vmax(t[0], t[1]), vmax(t[2], t[3])));
  if (Q) m = vmax(m, vmax(vmax(t[4], t[5]), vmax(t[6], t[7])));
  m = max32(max16(m));
  float e[4], l = 0.f;
#pragma unroll
  for (int k = 0; k < 4; ++k) { e[k] = __builtin_amdgcn_exp2f(sm[k] - m); l += e[k]; }
  l *= 0.5f;
#pragma unroll
  for (int k = 0; k < (Q ? 8 : 4); ++k) { t[k] = __builtin_amdgcn_exp2f(t[k] - m); l += t[k]; }
  l = sum32(sum16(l));
  const float rl = __builtin_amdgcn_rcpf(l);
  pt = u32x4{pk(t[0] * rl, t[1] * rl), pk(t[2] * rl, t[3] * rl), Q ? pk(t[4] * rl, t[5] * rl) : 0u, Q ? pk(t[6] * rl, t[7] * rl) : 0u};
  pm = u32x2{pk(e[0] * rl, e[1] * rl), pk(e[2] * rl, e[3] * rl)};
}

__device__ __forceinline__ void task_attn(const Params& P, int s, int cbg, LAS unsigned char* lds, int& tmo) {
  int tid = threadIdx.x;
  asm volatile("" : "+v"(tid));
  const int lane = tid & 63, wave = __builtin_amdgcn_readfirstlane(tid >> 6);
  const rsrc_t rs = ws_rsrc(P.ws);
  const int n = lane & 15, g = lane >> 4;
  LAS unsigned char* PT = lds + 8192;
  LAS unsigned char* PM = PT + 32768;
  LAS unsigned char* VS = PM + 16384;
  __syncthreads();
  {
    const unsigned fo = lane < 32 ? FL_T1V + (unsigned)lane * FS : (lane & 1) ? FL_T2 + (unsigned)s * FS : FL_F2 + (unsigned)(s >> 1) * FS;
    if (wave == 0) wait_lanes<24>(rs, fo, lane < 32 ? s + 1 : 1, tmo);
    BAR();
  }
  const int cbl = wave >> 1, hh8 = wave & 1, cb = 4 * cbg + cbl, qo = hh8;
  f32x4 sxx[2][3];
  u32x2 bf;
  float gate;
  {
    u32x4 vx[8];
#pragma unroll
    for (int k = 0; k < 8; ++k) vx[k] = ld16(rs, lane * 16, OFF_VX + (unsigned)((2 * wave + (k >> 2)) * 28 + 4 * cbg + (k & 3)) * 1024u + (unsigned)s * VX_S);
#pragma unroll
    for (int hh = 0; hh < 2; ++hh)
#pragma unroll
      for (int k = 0; k < 3; ++k) sxx[hh][k] = __builtin_bit_cast(f32x4, ld16(rs, lane * 16, OFF_SXX + (unsigned)((2 * wave + hh) * 3 + k) * 1024u + (unsigned)s * SXX_S));
    bf = ld8(rs, lane * 8, OFF_BF + (unsigned)(qo * 28 + cb) * 512u + (unsigned)s * BF_S);
    gate = __builtin_bit_cast(float, ld4(rs, (unsigned)n * 4u, OFF_G + (unsigned)(32 * s + 16 * qo) * 4u));
#pragma unroll
    for (int k = 0; k < 8; ++k) *(LAS u32x4*)(VS + ((2 * wave + (k >> 2)) * 4 + (k & 3)) * 1024 + lane * 16) = vx[k];
  }
  VMWAIT0();
  __syncthreads();
  if (s == NSEG - 1 && cbg == 0 && wave == 0) STAMP(P, 15);
  if (wave == 0) {
    const unsigned vo = FL_CH + (unsigned)(lane < 60 ? lane : 0) * FS;
    for (unsigned sp = 0;; ++sp) {
      asm volatile("" ::: "memory");
      const int v = (int)ld4(rs, vo, 0);
      if (__builtin_amdgcn_ballot_w64(v < s + 1) == 0ull) break;
      if (sp > SPIN_MAX || tmo) { tmo = 1; break; }
      const int v0 = __builtin_amdgcn_readfirstlane(v);
      const int far = min(s - v0 - 1, 16);
      for (int k = 0; k < far; ++k) __builtin_amdgcn_s_sleep(48);
      __builtin_amdgcn_s_sleep(4);
    }
  }
  BAR();
  if (s == NSEG - 1 && cbg == 0 && wave == 0) STAMP(P, 16);
  f32x4 smq[2][2];
  u32x2 vm[8];
  {
    u32x4 sp[2][2][2];
#pragma unroll
    for (int hh = 0; hh < 2; ++hh)
#pragma unroll
      for (int q = 0; q < 2; ++q)
#pragma unroll
        for (int hq = 0; hq < 2; ++hq)
          sp[hh][q][hq] = ld16(rs, (unsigned)((g & 1) * 16 + n) * 16u, OFF_KM + (unsigned)((2 * wave + hh) * 2 + hq) * 1024u + (unsigned)q * 512u + (unsigned)s * KM_S);
#pragma unroll
    for (int h = 0; h < 8; ++h) vm[h] = ld8(rs, lane * 8, OFF_VM + (unsigned)((8 * hh8 + h) * 28 + cb) * 512u + (unsigned)s * VM_S);
    SCHED();
#pragma unroll
    for (int hh = 0; hh < 2; ++hh)
#pragma unroll
      for (int q = 0; q < 2; ++q) smq[hh][q] = __builtin_bit_cast(f32x4, sp[hh][q][0]) + __builtin_bit_cast(f32x4, sp[hh][q][1]);
  }
  if (s == NSEG - 1 && cbg == 0 && wave == 0) STAMP(P, 17);
#pragma unroll
  for (int hh = 0; hh < 2; ++hh) {
    const int h = 2 * wave + hh;
    u32x4 pt;
    u32x2 pm;
    attn_softmax<0>(smq[hh][0], sxx[hh][0], sxx[hh][2], g, n, pt, pm);
    *(LAS u32x4*)(PT + (h * 2 + 0) * 1024 + lane * 16) = pt;
    *(LAS u32x2*)(PM + (h * 2 + 0) * 512 + lane * 8) = pm;
    attn_softmax<1>(smq[hh][1], sxx[hh][1], sxx[hh][2], g, n, pt, pm);
    *(LAS u32x4*)(PT + (h * 2 + 1) * 1024 + lane * 16) = pt;
    *(LAS u32x2*)(PM + (h * 2 + 1) * 512 + lane * 8) = pm;
  }
  __syncthreads();
  if (s == NSEG - 1 && cbg == 0 && wave == 0) STAMP(P, 18);
  f32x4 at[2] = {{0, 0, 0, 0}, {0, 0, 0, 0}}, am[2] = {{0, 0, 0, 0}, {0, 0, 0, 0}};
#pragma unroll
  for (int k = 0; k < 8; ++k) {
    const int h = 8 * hh8 + k;
    const bf16x8 vx = *(const LAS bf16x8*)(VS + (h * 4 + cbl) * 1024 + lane * 16);
#pragma unroll
    for (int q = 0; q < 2; ++q) {
      const bf16x8 pt = *(const LAS bf16x8*)(PT + (h * 2 + q) * 1024 + lane * 16);
      const u32x2 pm = *(const LAS u32x2*)(PM + (h * 2 + q) * 512 + lane * 8);
      at[q] = mfma32(vx, pt, at[q]);
      am[q] = mfma16(as_s4(vm[k]), as_s4(pm), am[q]);
    }
  }
  LAS f32x4* ex = (LAS f32x4*)lds;
  ex[(wave ^ 1) * 64 + lane] = hh8 ? at[0] + am[0] : at[1] + am[1];
  __syncthreads();
  const f32x4 acc = (hh8 ? at[1] + am[1] : at[0] + am[0]) + ex[wave * 64 + lane];
  const float gb = bflo(pk(gate, 0.f));
  const float og = bflo(pk(1.0f - gb, 0.f));
  const float bv[4] = {bflo(bf.x), bfhi(bf.x), bflo(bf.y), bfhi(bf.y)};
  float o[4];
#pragma unroll
  for (int k = 0; k < 4; ++k) {
    const float av = bflo(pk(acc[k], 0.f));
    o[k] = bflo(pk(gb * av, 0.f)) + bflo(pk(og * bv[k], 0.f));
  }
  const u32x2 ov = {pk(o[0], o[1]), pk(o[2], o[3])};
  *(u32x2*)(P.out + (size_t)(32 * s + 16 * qo + n) * DM + 16 * cb + 4 * g) = ov;
}

constexpr int NTASK = 32 * 17;
__device__ __forceinline__ void generic_wg(const Params& P, LAS unsigned char* lds) {
  int tmo = 0;
  LAS int* tk = (LAS int*)(lds + LDS_BYTES - 16);
  for (;;) {
    __syncthreads();
    if (threadIdx.x == 0) *tk = (int)__hip_atomic_fetch_add((GAS unsigned*)(P.ws + FL_Q), 1u, __ATOMIC_RELAXED, __HIP_MEMORY_SCOPE_AGENT);
    __syncthreads();
    const int t = __builtin_amdgcn_readfirstlane(*tk);
    if (t >= NTASK) { if (threadIdx.x < 64) LSTAMP(P, 768 + blockIdx.x); break; }
    int kind = 2, s = 0, c = 0;
    {
      int cnt = t, npre = 0, p = -1;
      for (;;) {
        const int lim = min(32, p < 0 ? 2 : (3 * (p + 1)) / 2 + 2);
        if (cnt < 3 * (lim - npre)) { const int pp = npre + cnt / 3, o = cnt % 3; kind = o == 0 ? 1 : 0; s = o == 0 ? pp : 2 * pp + o - 1; break; }
        cnt -= 3 * (lim - npre); npre = lim; ++p;
        if (cnt < 14) { kind = 2; s = 2 * p + (cnt >= 7 ? 1 : 0); c = cnt >= 7 ? cnt - 7 : cnt; break; }
        cnt -= 14;
      }
    }
    if (threadIdx.x < 64) STAMP(P, 1024 + 2 * t);
    if (kind == 0) task_sxx(P, s, lds, tmo);
    else if (kind == 1) task_ffn2(P, s, lds, tmo);
    else task_attn(P, s, c, lds, tmo);
    if (threadIdx.x < 64) STAMP(P, 1024 + 2 * t + 1);
  }
}

__global__ void __launch_bounds__(512, 2) main_kernel(Params P) {
  extern __shared__ __attribute__((aligned(16))) unsigned char smem[];
  LAS unsigned char* lds = (LAS unsigned char*)smem;
  const int b = blockIdx.x, r = b >> 3;
  const int x = (b + (int)*(const unsigned*)(P.ws + FL_ROT)) & 7;
  if (threadIdx.x < 64) { STAMP(P, 512 + b); LSTAMP(P, 512 + b); }
  if (x < 4) {
    if (x == 0 || x == 2) {
      const int idx = (x >> 1) * 16 + (r & 15);
      if (r < 16) {
        const int wave = __builtin_amdgcn_readfirstlane(threadIdx.x >> 6);
        if (wave < 7) score_main(P, idx, wave, lds); else score_sx(P, idx, lds);
      } else if (L2HELP) l2_help(P, OFF_WK + (unsigned)((idx >> 1) * 28 + 14 * (idx & 1)) * 14336u, 14336u, 14 * 6);
    } else if (r < 14) {
      value_wg(P, (x >> 1) * 14 + r, lds);
    } else if (x == 1 && r == 31) probe_pol(P);
    else if (r < 28 && L2HELP) l2_help(P, OFF_WV + (unsigned)((x >> 1) * 14 + r - 14) * 14336u, 28u * 14336u, 16 * 6);
  } else {
    const int y = x - 4;
    if (r < 8) stream_wg<0>(P, y * 8 + r, lds);
    else if (r < 16) stream_wg<1>(P, y * 8 + r - 8, lds);
    else if (r < 20) stream_wg<2>(P, y * 4 + r - 16, lds);
    else if (L2HELP) {
      const int j = r - 20, f0 = 112 * j;
      for (int w = f0 / 84; w * 84 < f0 + 112; ++w) {
        const int lo = max(f0, w * 84) - w * 84, hi = min(f0 + 112, w * 84 + 84) - w * 84, id = y * 8 + (w & 7);
        const unsigned base = (w < 8 ? OFF_WK : OFF_WV) + (unsigned)((id >> 1) * 28 + 14 * (id & 1)) * 14336u;
        l2_help(P, base + (unsigned)(lo / 6) * 14336u, 14336u, ((hi + 5) / 6 - lo / 6) * 6);
      }
    }
    generic_wg(P, lds);
  }
}

}

extern "C" void kernel_launch(void* const* d_in, const int* in_sizes, int n_in, void* d_out, int out_size, void* d_ws, size_t ws_size,
                              hipStream_t stream) {
  Params P;
  P.x = (const unsigned short*)d_in[0];
  P.mem0 = (const unsigned short*)d_in[1];
  P.Wq = (const unsigned short*)d_in[2];
  P.Wk = (const unsigned short*)d_in[3];
  P.Wv = (const unsigned short*)d_in[4];
  P.Wo = (const unsigned short*)d_in[5];
  P.Wg = (const unsigned short*)d_in[8];
  P.Wu = (const unsigned short*)d_in[9];
  P.Wd = (const unsigned short*)d_in[10];
  P.gw = (const unsigned short*)d_in[11];
  P.out = (unsigned short*)d_out;
  const uintptr_t base = ((uintptr_t)d_ws + ((1u << 21) - 1)) & ~(uintptr_t)((1u << 21) - 1);
  P.ws = (unsigned char*)base;
  (void)hipFuncSetAttribute((const void*)prep_kernel, hipFuncAttributeMaxDynamicSharedMemorySize, 57344);
  (void)hipFuncSetAttribute((const void*)main_kernel, hipFuncAttributeMaxDynamicSharedMemorySize, LDS_BYTES);
  hipLaunchKernelGGL(prep_kernel, dim3(256), dim3(512), 57344, stream, P);
  hipLaunchKernelGGL(main_kernel, dim3(256), dim3(512), LDS_BYTES, stream, P);
}
```

```cpp
#include <hip/hip_runtime.h>
#include <stdint.h>

namespace {

typedef __bf16 bf16x2 __attribute__((ext_vector_type(2)));
typedef __bf16 bf16x8 __attribute__((ext_vector_type(8)));
typedef __bf16 bf16x16 __attribute__((ext_vector_type(16)));
typedef short s16x4 __attribute__((ext_vector_type(4)));
typedef float f32x4 __attribute__((ext_vector_type(4)));
typedef unsigned u32x4 __attribute__((ext_vector_type(4)));
typedef unsigned u32x2 __attribute__((ext_vector_type(2)));
#define LAS __attribute__((address_space(3)))
#define GAS __attribute__((address_space(1)))
typedef __amdgpu_buffer_rsrc_t rsrc_t;

constexpr unsigned SENT = 0xFFFFFFFFu;
constexpr int NSEG = 64;
constexpr int DM = 448;
constexpr int AUX_SC1 = 16;

constexpr unsigned SZ_W = 16u * 28 * 14 * 1024;
constexpr unsigned OFF_WK = 0;
constexpr unsigned OFF_WV = OFF_WK + SZ_W;
constexpr unsigned OFF_KX = OFF_WV + SZ_W;
constexpr unsigned KX_S = 16u * 2 * 14 * 1024;
constexpr unsigned OFF_VX = OFF_KX + 64u * KX_S;
constexpr unsigned VX_S = 16u * 28 * 1024;
constexpr unsigned OFF_KM = OFF_VX + 64u * VX_S;
constexpr unsigned KM_S = 16u * 14 * 512;
constexpr unsigned OFF_VM = OFF_KM + 64u * KM_S;
constexpr unsigned VM_S = 16u * 28 * 512;
constexpr unsigned OFF_WD = OFF_VM + 64u * VM_S;
constexpr unsigned OFF_HF = OFF_WD + 28u * 56 * 1024;
constexpr unsigned HF_S = 2u * 112 * 512;
constexpr unsigned OFF_BF = OFF_HF + 64u * HF_S;
constexpr unsigned BF_S = 2u * 28 * 512;
constexpr unsigned OFF_SXX = OFF_BF + 64u * BF_S;
constexpr unsigned SXX_S = 16u * 3 * 1024;
constexpr unsigned OFF_XF = OFF_SXX + 64u * SXX_S;
constexpr unsigned XF_S = 28u * 1024;
constexpr unsigned OFF_G = OFF_XF + 64u * XF_S;
constexpr unsigned OFF_FL = OFF_G + 8192;
constexpr unsigned FS = 128;
constexpr unsigned FL_T1K = OFF_FL;
constexpr unsigned FL_T1V = FL_T1K + 32 * FS;
constexpr unsigned FL_F1 = FL_T1V + 32 * FS;
constexpr unsigned FL_CH = FL_F1 + 16 * FS;
constexpr unsigned FL_T2 = FL_CH + 64 * FS;
constexpr unsigned FL_F2 = FL_T2 + 64 * FS;
constexpr unsigned FL_POL = FL_F2 + 64 * FS;
constexpr unsigned FL_ROT = FL_POL + FS;
constexpr unsigned FL_Q = FL_POL - FS;
constexpr unsigned FL_COUNT = 32 + 32 + 16 + 64 + 64 + 64 + 1;
constexpr unsigned OFF_DBG = OFF_FL + 65536;
constexpr unsigned OFF_MB = 112u << 20;
constexpr unsigned WS_USED = OFF_MB + (8u << 20);
static_assert(OFF_DBG + (2u << 20) <= OFF_MB, "workspace map");
constexpr unsigned K_SM = 0, K_SX = 32768, K_M = 65536, K_REP = 16384, K_PROBE = 98304;

constexpr unsigned SPIN_MAX = 40000u;
constexpr int LDS_BYTES = 144 * 1024;

#define TUNE 0
struct Params {
  const unsigned short* x;
  const unsigned short* mem0;
  const unsigned short* Wq;
  const unsigned short* Wk;
  const unsigned short* Wv;
  const unsigned short* Wo;
  const unsigned short* Wg;
  const unsigned short* Wu;
  const unsigned short* Wd;
  const unsigned short* gw;
  unsigned short* out;
  unsigned char* ws;
};

#define D_SX 12
#define D_SM 2
#define D_MV 7
#define D_MS 28
#define PROBEDBG 0
#define K1PLAIN 0
#define NEWSCHED 1
#define POLX 0
#define OVERLAP 1
#define D_GAP 5
#define D_G2 0
#define D_BULK 4

__device__ __forceinline__ unsigned pk(float a, float b) {
  bf16x2 v = {(__bf16)a, (__bf16)b};
  return __builtin_bit_cast(unsigned, v);
}
__device__ __forceinline__ unsigned san(unsigned v) { return min(v, 0xFFFFFFFEu); }
__device__ __forceinline__ float bflo(unsigned v) { return __builtin_bit_cast(float, v << 16); }
__device__ __forceinline__ float bfhi(unsigned v) { return __builtin_bit_cast(float, v & 0xFFFF0000u); }
__device__ __forceinline__ float vmax(float a, float b) { return __builtin_elementwise_maximum(a, b); }
template <int N> __device__ __forceinline__ void delay_c() {
#pragma unroll 1
  for (int i = 0; i < N; ++i) __builtin_amdgcn_s_sleep(1);
}
__device__ __forceinline__ void delay_r(int n) {
#pragma unroll 1
  for (int i = 0; i < n; ++i) __builtin_amdgcn_s_sleep(1);
}
#define TUNE_DECL
#define DELAY(NAME, K) delay_c<NAME>()
#define TDP nullptr
__device__ __forceinline__ float max16(float v) {
  unsigned u = __builtin_bit_cast(unsigned, v);
  auto rr = __builtin_amdgcn_permlane16_swap(u, u, false, false);
  const unsigned r0 = rr[0], r1 = rr[1];
  return vmax(__builtin_bit_cast(float, r0), __builtin_bit_cast(float, r1));
}
__device__ __forceinline__ float max32(float v) {
  unsigned u = __builtin_bit_cast(unsigned, v);
  auto rr = __builtin_amdgcn_permlane32_swap(u, u, false, false);
  const unsigned r0 = rr[0], r1 = rr[1];
  return vmax(__builtin_bit_cast(float, r0), __builtin_bit_cast(float, r1));
}
__device__ __forceinline__ float sum16(float v) {
  unsigned u = __builtin_bit_cast(unsigned, v);
  auto rr = __builtin_amdgcn_permlane16_swap(u, u, false, false);
  const unsigned r0 = rr[0], r1 = rr[1];
  return __builtin_bit_cast(float, r0) + __builtin_bit_cast(float, r1);
}
__device__ __forceinline__ float sum32(float v) {
  unsigned u = __builtin_bit_cast(unsigned, v);
  auto rr = __builtin_amdgcn_permlane32_swap(u, u, false, false);
  const unsigned r0 = rr[0], r1 = rr[1];
  return __builtin_bit_cast(float, r0) + __builtin_bit_cast(float, r1);
}
__device__ __forceinline__ bf16x8 as_b8(u32x4 v) { return __builtin_bit_cast(bf16x8, v); }
__device__ __forceinline__ s16x4 as_s4(u32x2 v) { return __builtin_bit_cast(s16x4, v); }
__device__ __forceinline__ bf16x16 cat16(bf16x8 a, bf16x8 b) {
  return __builtin_shufflevector(a, b, 0, 1, 2, 3, 4, 5, 6, 7, 8, 9, 10, 11, 12, 13, 14, 15);
}
__device__ __forceinline__ f32x4 mfma32(bf16x8 a, bf16x8 b, f32x4 c) { return __builtin_amdgcn_mfma_f32_16x16x32_bf16(a, b, c, 0, 0, 0); }
__device__ __forceinline__ f32x4 mfma16(s16x4 a, s16x4 b, f32x4 c) { return __builtin_amdgcn_mfma_f32_16x16x16bf16_1k(a, b, c, 0, 0, 0); }
__device__ __forceinline__ f32x4 smf(bf16x8 a, bf16x16 b, f32x4 c, int idx) { return __builtin_amdgcn_smfmac_f32_16x16x64_bf16(a, b, c, idx, 0, 0); }
__device__ __forceinline__ u32x4 ld16(rsrc_t rs, unsigned v, unsigned s) { return __builtin_amdgcn_raw_buffer_load_b128(rs, v, s, AUX_SC1); }
__device__ __forceinline__ u32x2 ld8(rsrc_t rs, unsigned v, unsigned s) { return __builtin_amdgcn_raw_buffer_load_b64(rs, v, s, AUX_SC1); }
__device__ __forceinline__ unsigned ld4(rsrc_t rs, unsigned v, unsigned s) { return __builtin_amdgcn_raw_buffer_load_b32(rs, v, s, AUX_SC1); }
__device__ __forceinline__ void st16(u32x4 d, rsrc_t rs, unsigned v, unsigned s) {
  __builtin_amdgcn_raw_buffer_store_b128(d, rs, v, s, AUX_SC1);
  asm volatile("s_nop 1" : "+v"(d)::"memory");
}
__device__ __forceinline__ void st8(u32x2 d, rsrc_t rs, unsigned v, unsigned s) { __builtin_amdgcn_raw_buffer_store_b64(d, rs, v, s, AUX_SC1); }
__device__ __forceinline__ void st4(unsigned d, rsrc_t rs, unsigned v, unsigned s) { __builtin_amdgcn_raw_buffer_store_b32(d, rs, v, s, AUX_SC1); }
__device__ __forceinline__ rsrc_t ws_rsrc(unsigned char* ws) { return __builtin_amdgcn_make_buffer_rsrc(ws, 0, WS_USED, 0x00020000); }
__device__ __forceinline__ void glds16(const void* src, LAS unsigned char* dst) {
  __builtin_amdgcn_global_load_lds((const GAS unsigned*)src, (LAS unsigned*)dst, 16, 0, 0);
}
__device__ __forceinline__ u32x4 ws_desc(unsigned char* ws) {
  const unsigned long long a = (unsigned long long)ws;
  return u32x4{(unsigned)a, (unsigned)(a >> 32) & 0xFFFFu, WS_USED, 0x00020000u};
}
#define ALD16(dst, voff, desc, soff) asm volatile("buffer_load_dwordx4 %0, %1, %2, %3 offen sc1" : "=&v"(dst) : "v"(voff), "s"(desc), "s"(soff) : "memory")
#define ALD8(dst, voff, desc, soff) asm volatile("buffer_load_dwordx2 %0, %1, %2, %3 offen sc1" : "=&v"(dst) : "v"(voff), "s"(desc), "s"(soff) : "memory")
#define ALD4(dst, voff, desc, soff) asm volatile("buffer_load_dword %0, %1, %2, %3 offen sc1" : "=&v"(dst) : "v"(voff), "s"(desc), "s"(soff) : "memory")
#define KEEP(x) asm volatile("" ::"v"(x))
#define PINW(w) _Pragma("unroll") for (int k_ = 0; k_ < 7; ++k_) asm volatile("" : "+v"((w)[k_]))
#define BAR() do { asm volatile("s_waitcnt lgkmcnt(0)" ::: "memory"); __builtin_amdgcn_s_barrier(); asm volatile("" ::: "memory"); } while (0)
#define VMWAIT0() asm volatile("s_waitcnt vmcnt(0)" ::: "memory")
#define SCHED() __builtin_amdgcn_sched_barrier(0)
#define TIMING 0
#define LIGHT 0
#define LSTAMP(P, slot) do { } while (0)
#define DBG_DECL
#define DT(slot) do { } while (0)
#define DBG_END(s, last) do { } while (0)
#define DBG_OUT(P, wg, wave) do { } while (0)
#define STAMP(P, slot) do { } while (0)
#define CNT(P, slot, v) do { } while (0)

__device__ __forceinline__ bool any_sent4(u32x4 v) {
  unsigned m = max(max(v.x, v.y), max(v.z, v.w));
  return __builtin_amdgcn_ballot_w64(m == SENT) != 0ull;
}
__device__ __forceinline__ bool any_sent2(u32x2 a, u32x2 b) {
  unsigned m = max(max(a.x, a.y), max(b.x, b.y));
  return __builtin_amdgcn_ballot_w64(m == SENT) != 0ull;
}
__device__ __forceinline__ bool any_sent8(u32x4 a, u32x4 b) {
  unsigned m = max(max(max(a.x, a.y), max(a.z, a.w)), max(max(b.x, b.y), max(b.z, b.w)));
  return __builtin_amdgcn_ballot_w64(m == SENT) != 0ull;
}
__device__ __forceinline__ unsigned mbase(int s, int pol) {
  return OFF_MB + (unsigned)s * 131072u + (((unsigned)pol >> (s >> 4)) & 1u) * 8192u;
}
__device__ __forceinline__ int wait_flags2(rsrc_t rs, unsigned off0, unsigned off1, int need, int& tmo) {
  const int lane = threadIdx.x & 63;
  const unsigned vo = (lane & 1) ? off1 : off0;
  int lo = 0;
  for (unsigned sp = 0;; ++sp) {
    asm volatile("" ::: "memory");
    int v = (int)ld4(rs, vo, 0);
    lo = min(__builtin_amdgcn_readlane(v, 0), __builtin_amdgcn_readlane(v, 1));
    if (lo >= need) break;
    if (sp > SPIN_MAX || tmo) { tmo = 1; lo = NSEG + 1; break; }
    __builtin_amdgcn_s_sleep(4);
  }
  return lo;
}

__global__ void __launch_bounds__(512, 2) prep_kernel(Params P) {
  extern __shared__ __attribute__((aligned(16))) unsigned char smem[];
  LAS unsigned char* lds = (LAS unsigned char*)smem;
  const int tid = threadIdx.x, lane = tid & 63, wave = __builtin_amdgcn_readfirstlane(tid >> 6);
  const int b = blockIdx.x, xcd = b & 7, j = b >> 3;
  const int wh = xcd * 4 + (j >> 3), which = wh >> 4, h = wh & 15, rb = (j >> 2) & 1, cq = j & 3;
  const int n = lane & 15, g = lane >> 4;
  const rsrc_t rs = ws_rsrc(P.ws);
  if (b == 0 && wave == 0) { STAMP(P, 8); LSTAMP(P, 8); }
  if (wave == 0) LSTAMP(P, 2816 + b);

  if (which == 0) {
#pragma unroll
    for (int k = 0; k < 7; ++k) {
      const int fr = wave * 7 + k;
      const int ct = 7 * cq + (fr >> 3), ke = fr & 7;
      const int dp = 32 * (ct >> 1) + 8 * (n >> 2) + 4 * (ct & 1) + (n & 3);
      glds16(P.Wq + ((size_t)(dp * 16 + h) * 256 + 32 * ke + 8 * g), lds + fr * 1024);
    }
  } else {
#pragma unroll
    for (int k = 0; k < 7; ++k) {
      const int i = (wave * 7 + k) * 64 + lane;
      const int e = i / 14, ch = i - e * 14;
      glds16(P.Wo + ((size_t)(h * 256 + e) * 448 + 112 * cq + 8 * ch), lds + (wave * 7 + k) * 1024);
    }
  }
  if (wave == 7) {
    u32x4 xv[8];
    unsigned short wv[7][8];
    unsigned mv[4];
    if (b >= 8) {
#pragma unroll
      for (int k = 0; k < 8; ++k) {
        const int fr = min(b - 8 + 248 * k, 64 * 28 - 1), s = fr / 28, kq = fr - s * 28, q = kq >= 14 ? 1 : 0, k2 = kq - 14 * q;
        xv[k] = *(const u32x4*)(P.x + (size_t)(32 * s + 16 * q + n) * DM + 32 * k2 + 8 * g);
      }
#pragma unroll
      for (int k = 0; k < 7; ++k) {
        const int fr = min(b - 8 + 248 * k, 28 * 56 - 1), cb = fr / 56, u = fr - cb * 56;
#pragma unroll
        for (int jj = 0; jj < 8; ++jj) wv[k][jj] = P.Wd[(size_t)(32 * u + 16 * (jj >> 2) + 4 * g + (jj & 3)) * DM + 16 * cb + n];
      }
    } else if (b < 7) {
#pragma unroll
      for (int k = 0; k < 4; ++k) {
        const int i = k * 64 + lane, gg = (i >> 6) & 3, hi = (i >> 5) & 1, r = (i >> 2) & 7, p = i & 3;
        mv[k] = *(const unsigned*)(P.mem0 + r * DM + 64 * b + 16 * gg + 4 * p + 2 * hi);
      }
    }
    VMWAIT0();
    __syncthreads();
    if (b >= 8) {
#pragma unroll
      for (int k = 0; k < 8; ++k) {
        const int fr = b - 8 + 248 * k;
        if (fr < 64 * 28) st16(xv[k], rs, (unsigned)lane * 16u, OFF_XF + (unsigned)fr * 1024u);
      }
#pragma unroll
      for (int k = 0; k < 7; ++k) {
        const int fr = b - 8 + 248 * k;
        const u32x4 o = {wv[k][0] | ((unsigned)wv[k][1] << 16), wv[k][2] | ((unsigned)wv[k][3] << 16), wv[k][4] | ((unsigned)wv[k][5] << 16), wv[k][6] | ((unsigned)wv[k][7] << 16)};
        if (fr < 28 * 56) st16(o, rs, (unsigned)lane * 16u, OFF_WD + (unsigned)fr * 1024u);
      }
    } else if (b < 7) {
#pragma unroll
      for (int k = 0; k < 4; ++k)
#pragma unroll
        for (int c = 0; c < 4; ++c) st4(san(mv[k]), rs, (unsigned)(k * 64 + lane) * 4u, OFF_MB + K_M + (unsigned)c * 8192u + (unsigned)b * 1024u);
    }
  }
  bf16x8 a[2][8];
  const int kk = 7 * rb + (wave < 7 ? wave : 0);
  if (wave < 7) {
    const unsigned short* W1 = which ? P.Wv : P.Wk;
#pragma unroll
    for (int T = 0; T < 2; ++T) {
      const int d = 32 * kk + 8 * (n >> 2) + 4 * T + (n & 3);
      const unsigned short* p = W1 + ((size_t)(d * 16 + h) * 256 + 8 * g);
#pragma unroll
      for (int ke = 0; ke < 8; ++ke) a[T][ke] = *(const bf16x8*)(p + 32 * ke);
    }
    VMWAIT0();
    __syncthreads();
  }
  if (b == 0 && wave == 0) STAMP(P, 10);
  if (wave == 0) LSTAMP(P, 2560 + b);
  {
    const u32x4 sv = {SENT, SENT, SENT, SENT};
#pragma unroll
    for (int k = 0; k < 4; ++k) {
      const unsigned o = (unsigned)b * 32768u + (unsigned)(k * 512 + tid) * 16u;
      if (!(o >= K_M && o < K_M + 32768u)) st16(sv, rs, OFF_MB + o, 0);
    }
    if (b == 0 && tid == 0) *(unsigned*)(P.ws + FL_ROT) = (unsigned)__builtin_amdgcn_s_getreg(20 | (3 << 11)) & 7u;
    if (b == 3 && tid < (int)FL_COUNT) st4(tid == (int)FL_COUNT - 1 ? SENT : 0u, rs, OFF_FL + (unsigned)tid * FS, 0);
  }
  if (wave == 7) { LSTAMP(P, 2048 + b); return; }
  if (b == 0 && wave == 0) STAMP(P, 11);
  f32x4 acc[2][7];
#pragma unroll
  for (int c = 0; c < 7; ++c) { acc[0][c] = f32x4{0, 0, 0, 0}; acc[1][c] = f32x4{0, 0, 0, 0}; }
  if (which == 0) {
#pragma unroll
    for (int ke = 0; ke < 8; ++ke)
#pragma unroll
      for (int c = 0; c < 7; ++c) {
        const bf16x8 bb = *(const LAS bf16x8*)(lds + (c * 8 + ke) * 1024 + lane * 16);
        acc[0][c] = mfma32(a[0][ke], bb, acc[0][c]);
        acc[1][c] = mfma32(a[1][ke], bb, acc[1][c]);
      }
  } else {
    const LAS unsigned char* tb = lds + (8 * g + (n >> 2)) * 224 + (n & 3) * 8;
#pragma unroll
    for (int ke = 0; ke < 8; ++ke)
#pragma unroll
      for (int c = 0; c < 7; ++c) {
        typedef short v4s __attribute__((ext_vector_type(4)));
        const v4s lo = __builtin_amdgcn_ds_read_tr16_b64_v4i16((LAS v4s*)(tb + ke * 32 * 224 + c * 32));
        const v4s hi = __builtin_amdgcn_ds_read_tr16_b64_v4i16((LAS v4s*)(tb + ke * 32 * 224 + 4 * 224 + c * 32));
        const bf16x8 bb = __builtin_bit_cast(bf16x8, __builtin_shufflevector(lo, hi, 0, 1, 2, 3, 4, 5, 6, 7));
        acc[0][c] = mfma32(a[0][ke], bb, acc[0][c]);
        acc[1][c] = mfma32(a[1][ke], bb, acc[1][c]);
      }
  }
  const float sc = which ? 1.0f : 0.0625f * 1.4426950408889634f;
  const unsigned dst = (which ? OFF_WV : OFF_WK) + (unsigned)((h * 28 + 7 * cq) * 14 + kk) * 1024u;
#pragma unroll
  for (int c = 0; c < 7; ++c) {
    const f32x4 u = acc[0][c] * sc, v = acc[1][c] * sc;
    const u32x4 o = {pk(u[0], u[1]), pk(u[2], u[3]), pk(v[0], v[1]), pk(v[2], v[3])};
    st16(o, rs, (unsigned)lane * 16u, dst + (unsigned)c * 14u * 1024u);
  }
  if (b == 0 && wave == 0) { STAMP(P, 9); LSTAMP(P, 9); }
  if (wave == 0) LSTAMP(P, 2304 + b);
}

__device__ __forceinline__ u32x4 gather_m(const int* td_, u32x4 dsc, unsigned voff, unsigned so0, LAS unsigned char* dst, bool act, int& tmo, unsigned& nfail) {
  u32x4 va, vb;
  ALD16(va, voff, dsc, so0);
  const unsigned so1 = so0 + K_REP;
  if (OVERLAP) {
    DELAY(D_GAP, 4);
    ALD16(vb, voff, dsc, so1);
    asm volatile("s_waitcnt vmcnt(1)" : "+v"(va)::"memory");
  } else {
    vb = u32x4{0, 0, 0, 0};
    asm volatile("s_waitcnt vmcnt(0)" : "+v"(va)::"memory");
  }
  if (__builtin_expect(any_sent4(va), 0)) {
    if (TIMING) ++nfail;
    if (!OVERLAP) ALD16(vb, voff, dsc, so1);
    asm volatile("s_waitcnt vmcnt(0)" : "+v"(vb)::"memory");
    va = vb;
    for (unsigned sp = 2; any_sent4(va); ++sp) {
      asm volatile("" ::: "memory");
      const unsigned r = sp % 3u, so = r == 2u ? (so0 ^ 8192u) : so0 + r * K_REP;
      ALD16(va, voff, dsc, so);
      asm volatile("s_waitcnt vmcnt(0)" : "+v"(va)::"memory");
      if (sp > SPIN_MAX || tmo) { tmo = 1; break; }
    }
  }
  if (act) *(LAS u32x4*)dst = va;
  return vb;
}

__device__ __forceinline__ void probe_pol(const Params& P) {
  if (threadIdx.x >= 64) return;
  const rsrc_t rs = ws_rsrc(P.ws);
  __builtin_amdgcn_s_sleep(64);
  unsigned pol = 0;
  for (int round = 0; round < 3; ++round) {
    int dsum = 0;
#pragma unroll
    for (int p = 0; p < 4; ++p) {
      unsigned t[2];
#pragma unroll
      for (int cls = 0; cls < 2; ++cls) {
        const unsigned off = OFF_MB + (unsigned)(p * 16 + round) * 131072u + K_PROBE + (unsigned)cls * 8192u;
        const unsigned long long c0 = __builtin_amdgcn_s_memtime();
        unsigned v = ld4(rs, 0, off);
        asm volatile("s_waitcnt vmcnt(0)" : "+v"(v)::"memory");
        const unsigned long long c1 = __builtin_amdgcn_s_memtime();
        t[cls] = (unsigned)(c1 - c0);
      }
      const int d = max(-300, min(300, (int)t[0] - (int)t[1]));
      dsum += (p & 1) ? -d : d;
    }
    pol = 0;
#pragma unroll
    for (int p = 0; p < 4; ++p) pol |= (((dsum > 0) ? 1u : 0u) ^ (unsigned)(p & 1)) << p;
    if (dsum > 450 || dsum < -450) break;
  }
  if (threadIdx.x == 0) {
    st4(pol, rs, FL_POL, 0);
  }
}
__device__ __forceinline__ int get_pol(rsrc_t rs, int& tmo) {
  unsigned v;
  for (unsigned sp = 0;; ++sp) {
    asm volatile("" ::: "memory");
    v = (unsigned)__builtin_amdgcn_readfirstlane((int)ld4(rs, 0, FL_POL));
    if (v != SENT) break;
    if (sp > SPIN_MAX) { tmo = 1; v = 0; break; }
    __builtin_amdgcn_s_sleep(4);
  }
  return (int)v;
}

__device__ __forceinline__ void score_main(const Params& P, int idx, int wave, LAS unsigned char* lds) {
  const int lane = threadIdx.x & 63;
  const int h = idx >> 1, q = idx & 1;
  const rsrc_t rs = ws_rsrc(P.ws);
  LAS unsigned char* img = lds;
  LAS float* red = (LAS float*)(lds + 8192);
  const int g = lane >> 4, n = lane & 15;
  const int idxv = (n & 8) ? 0xEEEE : 0x4444;
  s16x4 E;
#pragma unroll
  for (int jj = 0; jj < 4; ++jj) E[jj] = (((4 * g + jj) & 7) == n) ? (short)0x3F80 : (short)0;
  int tmo = 0;
  unsigned nfail = 0;
  if (idx == 0 && wave == 0) STAMP(P, 0);
  bf16x16 w0[7], w1[7];
  const int Pp = 7 * q + wave;
  {
    const unsigned char* b0 = P.ws + OFF_WK + ((size_t)(h * 28 + 2 * Pp) * 14) * 1024 + lane * 16;
#pragma unroll
    for (int ks = 0; ks < 7; ++ks) {
      w0[ks] = cat16(*(const bf16x8*)(b0 + (2 * ks) * 1024), *(const bf16x8*)(b0 + (2 * ks + 1) * 1024));
      w1[ks] = cat16(*(const bf16x8*)(b0 + (14 + 2 * ks) * 1024), *(const bf16x8*)(b0 + (15 + 2 * ks) * 1024));
    }
  }
  const int pol = get_pol(rs, tmo) ^ POLX;
  const unsigned gloc = (unsigned)(wave * 56 + (lane < 56 ? lane : 55)) * 16u;
  const unsigned gvoff = gloc + K_M;
  LAS unsigned char* gdst = img + gloc;
  const LAS unsigned char* afp = img + lane * 16;
  const LAS unsigned char* mp = img + (Pp >> 1) * 1024 + (2 * (Pp & 1) + (g >> 1)) * 256 + (n & 7) * 16 + (g & 1) * 8;
  const unsigned mpa = (unsigned)(size_t)mp;
  LAS float* redw = red + wave * 64 + n * 8 + 4 * g;
  const unsigned xfv = OFF_XF + (unsigned)Pp * 1024u + lane * 16, xfv1 = xfv + 14 * 1024;
  LAS float* red2 = (LAS float*)(lds + 12288);
  LAS float* red2w = red2 + wave * 256 + (g & 1) * 64 + n * 4;
  const LAS float* red2r = red2 + lane * 4;
  const unsigned sxmv = OFF_KM + (unsigned)idx * 1024u + lane * 16;
  const unsigned smv = K_SM + (unsigned)idx * 256u + lane * 4u;
  PINW(w0); PINW(w1);
  const u32x4 dsc = ws_desc(P.ws);
  u32x4 xf0, xf1;
  { const unsigned so = 0; ALD16(xf0, xfv, dsc, so); ALD16(xf1, xfv1, dsc, so); }
  TUNE_DECL
  DBG_DECL
  for (int s = 0; s < NSEG; ++s) {
    const unsigned mb = mbase(s, pol);
    if (s > 0) DELAY(D_MS, 0);
    DT(0);
    const u32x4 pend = gather_m(TDP, dsc, gvoff, mb, gdst, lane < 56, tmo, nfail);
    DT(1);
    BAR();
    DT(2);
    f32x4 c0, c1;
    u32x4 mf;
    {
      u32x2 mlo, mhi;
      asm volatile("ds_read2_b32 %0, %2 offset0:0 offset1:32\n\tds_read2_b32 %1, %2 offset0:1 offset1:33" : "=&v"(mlo), "=&v"(mhi) : "v"(mpa) : "memory");
      bf16x8 a[7];
#pragma unroll
      for (int ks = 0; ks < 7; ++ks) a[ks] = *(const LAS bf16x8*)(afp + ks * 1024);
      SCHED();
      c0 = f32x4{0, 0, 0, 0}; c1 = f32x4{0, 0, 0, 0};
#pragma unroll
      for (int ks = 0; ks < 7; ++ks) {
        c0 = smf(a[ks], w0[ks], c0, idxv);
        c1 = smf(a[ks], w1[ks], c1, idxv);
      }
      SCHED();
      asm volatile("s_waitcnt lgkmcnt(0)" : "+v"(mlo), "+v"(mhi)::"memory");
      mf = u32x4{mlo.x, mlo.y, mhi.x, mhi.y};
    }
    SCHED();
    const u32x2 t0 = {pk(c0[0], c0[1]), pk(c0[2], c0[3])}, t1 = {pk(c1[0], c1[1]), pk(c1[2], c1[3])};
    const f32x4 z = {0, 0, 0, 0};
    SCHED();
    const f32x4 y0 = mfma16(as_s4(t0), E, z), y1 = mfma16(as_s4(t1), E, z);
    SCHED();
    const u32x4 kf = {pk(y0[0], y0[1]), pk(y0[2], y0[3]), pk(y1[0], y1[1]), pk(y1[2], y1[3])};
    const f32x4 S = mfma32(as_b8(kf), as_b8(mf), z);
    if (n < 8 && g < 2) *(LAS f32x4*)redw = S;
    VMWAIT0();
    KEEP(pend);
    DT(3);
    BAR();
    DT(4);
    if (wave == 0 && s < NSEG - 1) {
      const float v0 = red[lane], v1 = red[64 + lane], v2 = red[128 + lane], v3 = red[192 + lane], v4 = red[256 + lane], v5 = red[320 + lane], v6 = red[384 + lane];
      const float v = ((v0 + v1) + (v2 + v3)) + ((v4 + v5) + v6);
      const unsigned o = san(__builtin_bit_cast(unsigned, v));
      st4(o, rs, smv, mb);
      st4(o, rs, smv + K_REP, mb);
      st4(o, rs, smv, mb ^ 8192u);
    }
    DT(5);
    if (idx == 0 && wave == 2 && (s & 7) == 0) LSTAMP(P, 64 + s);
    DBG_END(s, 5);
    asm volatile("" : "+v"(xf0), "+v"(xf1));
    {
      const f32x4 d0 = mfma32(as_b8(kf), as_b8(xf0), z), d1 = mfma32(as_b8(kf), as_b8(xf1), z);
      if (g < 2) { *(LAS f32x4*)red2w = d0; *(LAS f32x4*)(red2w + 128) = d1; }
    }
    BAR();
    if (wave == 1) {
      f32x4 v = *(const LAS f32x4*)red2r;
#pragma unroll
      for (int w = 1; w < 7; ++w) v += *(const LAS f32x4*)(red2r + w * 256);
      st16(__builtin_bit_cast(u32x4, v), rs, sxmv, (unsigned)s * KM_S);
    }
    if (s < NSEG - 1) DELAY(D_BULK, 5);
    { const unsigned so = (unsigned)min(s + 1, NSEG - 1) * XF_S; ALD16(xf0, xfv, dsc, so); ALD16(xf1, xfv1, dsc, so); }
  }
  DBG_OUT(P, idx, wave);
  VMWAIT0();
  BAR();
  CNT(P, 0, nfail);
  if (idx == 0 && wave == 0) { STAMP(P, 1); LSTAMP(P, 1); }
}

__device__ __forceinline__ void score_sx(const Params& P, int idx, LAS unsigned char* lds) {
  const int lane = threadIdx.x & 63;
  const int wave = 7;
  const int h = idx >> 1, q = idx & 1;
  const rsrc_t rs = ws_rsrc(P.ws);
  LAS unsigned char* img = lds;
  const int g = lane >> 4, n = lane & 15;
  const int idxv = (n & 8) ? 0xEEEE : 0x4444;
  s16x4 E;
#pragma unroll
  for (int jj = 0; jj < 4; ++jj) E[jj] = (((4 * g + jj) & 7) == n) ? (short)0x3F80 : (short)0;
  int tmo = 0;
  unsigned nfail = 0;
  const int pol = get_pol(rs, tmo) ^ POLX;
  if (idx == 0) STAMP(P, 2);
  const unsigned f0 = FL_T1K + (unsigned)(h * 2) * FS, f1 = f0 + FS;
  int known = wait_flags2(rs, f0, f1, 1, tmo);
  if (idx == 0) STAMP(P, 3);
  int fl = 0;
  const unsigned flv = (lane & 1) ? f1 : f0;
  u32x4 kx[14];
  const unsigned kxv = OFF_KX + (unsigned)((h * 2 + q) * 14) * 1024u + lane * 16;
  const u32x4 dsc = ws_desc(P.ws);
  const unsigned kxa[4] = {kxv, kxv + 4096, kxv + 8192, kxv + 12288};
#define KXLD(k, so) asm volatile("buffer_load_dwordx4 %0, %1, %2, %3 offen offset:%c4 sc1" : "=&v"(kx[k]) : "v"(kxa[(k) >> 2]), "s"(dsc), "s"(so), "i"(((k) & 3) * 1024) : "memory")
  {
    const unsigned so = 0;
    KXLD(0, so); KXLD(1, so); KXLD(2, so); KXLD(3, so); KXLD(4, so); KXLD(5, so); KXLD(6, so);
    KXLD(7, so); KXLD(8, so); KXLD(9, so); KXLD(10, so); KXLD(11, so); KXLD(12, so); KXLD(13, so);
  }
  const unsigned gloc = (unsigned)(wave * 56 + (lane < 56 ? lane : 55)) * 16u;
  const unsigned gvoff = gloc + K_M;
  LAS unsigned char* gdst = img + gloc;
  const LAS unsigned char* afp = img + lane * 16;
  const unsigned sxv = K_SX + (unsigned)idx * 256u + (unsigned)(g * 8 + (n & 7)) * 8u;
  const unsigned chf = FL_CH + (unsigned)idx * FS;
  __builtin_amdgcn_s_setprio(3);
  TUNE_DECL
  for (int s = 0; s < NSEG; ++s) {
    const unsigned mb = mbase(s, pol);
    if (s > 0) DELAY(D_MS, 0);
    const u32x4 pend = gather_m(TDP, dsc, gvoff, mb, gdst, lane < 56, tmo, nfail);
    BAR();
    {
      bf16x8 a[7];
#pragma unroll
      for (int ks = 0; ks < 7; ++ks) a[ks] = *(const LAS bf16x8*)(afp + ks * 1024);
      SCHED();
      f32x4 c0 = {0, 0, 0, 0};
#pragma unroll
      for (int ks = 0; ks < 7; ++ks) c0 = smf(a[ks], cat16(as_b8(kx[2 * ks]), as_b8(kx[2 * ks + 1])), c0, idxv);
      VMWAIT0();
      KEEP(pend);
      const u32x2 t0 = {pk(c0[0], c0[1]), pk(c0[2], c0[3])};
      const f32x4 z = {0, 0, 0, 0};
      const f32x4 y0 = mfma16(as_s4(t0), E, z);
      const u32x2 o = {san(pk(y0[0], y0[1])), san(pk(y0[2], y0[3]))};
      if (n < 8 && s < NSEG - 1) {
        st8(o, rs, sxv, mb);
        st8(o, rs, sxv + K_REP, mb);
        st8(o, rs, sxv, mb ^ 8192u);
      }
      if (lane == 0) st4((unsigned)s, rs, chf, 0);
      SCHED();
    }
    BAR();
    BAR();
    if (s < NSEG - 1) DELAY(D_BULK, 5);
    const int sn = min(s + 1, NSEG - 2);
    asm volatile("" : "+v"(fl));
    known = max(known, min(__builtin_amdgcn_readlane(fl, 0), __builtin_amdgcn_readlane(fl, 1)));
    if (__builtin_expect(known <= sn, 0)) { known = wait_flags2(rs, f0, f1, sn + 1, tmo); CNT(P, 4, 1); }
    { const unsigned z = 0; ALD4(fl, flv, dsc, z); }
    {
      const unsigned so = (unsigned)sn * KX_S;
      KXLD(0, so); KXLD(1, so); KXLD(2, so); KXLD(3, so); KXLD(4, so); KXLD(5, so); KXLD(6, so);
      KXLD(7, so); KXLD(8, so); KXLD(9, so); KXLD(10, so); KXLD(11, so); KXLD(12, so); KXLD(13, so);
    }
    SCHED();
  }
  VMWAIT0();
  BAR();
  if (lane == 0) st4((unsigned)NSEG, rs, chf, 0);
  CNT(P, 0, nfail);
}

__device__ __forceinline__ void value_wg(const Params& P, int cb, LAS unsigned char* lds) {
  const int tid = threadIdx.x, lane = tid & 63, wave = __builtin_amdgcn_readfirstlane(tid >> 6);
  const rsrc_t rs = ws_rsrc(P.ws);
  LAS unsigned char* img = lds;
  LAS float* red = (LAS float*)(lds + 8192);
  const int g = lane >> 4, n = lane & 15, sel = n >> 3, i = n & 7;
  const int idxv = (n & 8) ? 0xEEEE : 0x4444;
  const int h0 = 2 * wave, hs = h0 + sel;
  int tmo = 0;
  unsigned nfail = 0, nfx = 0, nfm = 0;
  bf16x16 w0[7], w1[7];
  {
    const unsigned char* b0 = P.ws + OFF_WV + ((size_t)(h0 * 28 + cb) * 14) * 1024 + lane * 16;
    const unsigned char* b1 = b0 + (size_t)28 * 14 * 1024;
#pragma unroll
    for (int ks = 0; ks < 7; ++ks) {
      w0[ks] = cat16(*(const bf16x8*)(b0 + (2 * ks) * 1024), *(const bf16x8*)(b0 + (2 * ks + 1) * 1024));
      w1[ks] = cat16(*(const bf16x8*)(b1 + (2 * ks) * 1024), *(const bf16x8*)(b1 + (2 * ks + 1) * 1024));
    }
  }
  const int pol = get_pol(rs, tmo) ^ POLX;
  const unsigned fa = FL_T1V + (unsigned)(h0 * 2 + (cb >= 14 ? 1 : 0)) * FS, fb = fa + 2 * FS;
  int known = 0;
  const unsigned vxv = OFF_VX + (unsigned)(h0 * 28 + cb) * 1024u + lane * 16, vxv1 = vxv + 28 * 1024;
  const unsigned flv = (lane & 1) ? fb : fa;
  const unsigned gloc = (unsigned)(wave * 56 + (lane < 56 ? lane : 55)) * 16u;
  const unsigned gvoff = gloc + K_M;
  LAS unsigned char* gdst = img + gloc;
  const LAS unsigned char* afp = img + lane * 16;
  const unsigned vmv = OFF_VM + (unsigned)(h0 * 28 + cb) * 512u + lane * 8;
  const unsigned sxv = K_SX + (unsigned)(hs * 2) * 256u + (unsigned)(g * 8 + i) * 8u;
  const unsigned smv = K_SM + (unsigned)(hs * 2) * 256u + (unsigned)(i * 8 + 4 * (g & 1)) * 4u;
  LAS float* redw = red + wave * 128 + i * 16 + 4 * g;
  const LAS float* redr = red + ((lane >> 2) & 7) * 16 + 4 * (lane & 3) + 2 * (lane >> 5);
  const unsigned mv = K_M + (unsigned)cb * 256u + lane * 4u;
  const unsigned chf = FL_CH + (unsigned)(32 + cb) * FS;
  u32x2 vm0 = {0, 0}, vm1 = {0, 0};
  PINW(w0); PINW(w1);
  const u32x4 dsc = ws_desc(P.ws);
  TUNE_DECL
  DBG_DECL
  for (int s = 0; s < NSEG - 1; ++s) {
    const unsigned mb = mbase(s, pol), mbn = mbase(s + 1, pol);
    if (s > 0) DELAY(D_MV, 1);
    DT(0);
    const u32x4 pend = gather_m(TDP, dsc, gvoff, mb, gdst, lane < 56, tmo, nfail);
    DT(1);
    BAR();
    if (__builtin_expect(known <= s, 0)) { known = wait_flags2(rs, fa, fb, s + 1, tmo); CNT(P, 5, 1); }
    bf16x8 a[7];
#pragma unroll
    for (int ks = 0; ks < 7; ++ks) a[ks] = *(const LAS bf16x8*)(afp + ks * 1024);
    u32x4 vx0, vx1;
    {
      const unsigned so = (unsigned)s * VX_S;
      ALD16(vx0, vxv, dsc, so);
      ALD16(vx1, vxv1, dsc, so);
    }
    st8(vm0, rs, vmv, (unsigned)max(s - 1, 0) * VM_S);
    st8(vm1, rs, vmv + 28 * 512, (unsigned)max(s - 1, 0) * VM_S);
    if (wave == 1 && lane == 0) st4((unsigned)max(s - 1, 0), rs, chf, 0);
    int fl;
    { const unsigned z = 0; ALD4(fl, flv, dsc, z); }
    SCHED();
    DELAY(D_SX, 2);
    u32x2 x0, x1, y0, y1;
    u32x4 ua, ub, wa, wb;
    const unsigned sxv1 = sxv + 256, smv1 = smv + 256, mb1 = mb + K_REP;
    ALD8(x0, sxv, dsc, mb);
    ALD8(x1, sxv1, dsc, mb);
    SCHED();
    f32x4 c0 = {0, 0, 0, 0}, c1 = {0, 0, 0, 0};
#pragma unroll
    for (int ks = 0; ks < 7; ++ks) {
      c0 = smf(a[ks], w0[ks], c0, idxv);
      c1 = smf(a[ks], w1[ks], c1, idxv);
    }
    vm0 = u32x2{pk(c0[0], c0[1]), pk(c0[2], c0[3])};
    vm1 = u32x2{pk(c1[0], c1[1]), pk(c1[2], c1[3])};
    SCHED();
    DT(2);
    delay_c<D_G2>();
    ALD8(y0, sxv, dsc, mb1);
    ALD8(y1, sxv1, dsc, mb1);
    DELAY(D_SM, 3);
    ALD16(ua, smv, dsc, mb);
    ALD16(ub, smv1, dsc, mb);
    asm volatile("s_waitcnt vmcnt(4)" : "+v"(x0), "+v"(x1), "+v"(vx0), "+v"(vx1), "+v"(fl)::"memory");
    KEEP(pend);
    known = max(known, min(__builtin_amdgcn_readlane(fl, 0), __builtin_amdgcn_readlane(fl, 1)));
    if (__builtin_expect(any_sent2(x0, x1), 0)) {
      if (TIMING) ++nfx;
      asm volatile("s_waitcnt vmcnt(2)" : "+v"(y0), "+v"(y1)::"memory");
      x0 = y0; x1 = y1;
      for (unsigned sp = 2; any_sent2(x0, x1); ++sp) {
        asm volatile("" ::: "memory");
        const unsigned r = sp % 3u, so = r == 2u ? (mb ^ 8192u) : mb + r * K_REP;
        ALD8(x0, sxv, dsc, so);
        ALD8(x1, sxv1, dsc, so);
        asm volatile("s_waitcnt vmcnt(0)" : "+v"(x0), "+v"(x1)::"memory");
        if (sp > SPIN_MAX || tmo) { tmo = 1; break; }
      }
    }
    DT(3);
    SCHED();
    float t[8] = {bflo(x0.x), bfhi(x0.x), bflo(x0.y), bfhi(x0.y), bflo(x1.x), bfhi(x1.x), bflo(x1.y), bfhi(x1.y)};
    float m1 = vmax(vmax(vmax(t[0], t[1]), vmax(t[2], t[3])), vmax(vmax(t[4], t[5]), vmax(t[6], t[7])));
    m1 = max32(max16(m1));
    float l1 = 0.f;
#pragma unroll
    for (int k = 0; k < 8; ++k) { t[k] = __builtin_amdgcn_exp2f(t[k] - m1); l1 += t[k]; }
    SCHED();
    ALD16(wa, smv, dsc, mb1);
    ALD16(wb, smv1, dsc, mb1);
    SCHED();
    l1 = sum32(sum16(l1));
    const u32x4 pb = {pk(t[0], t[1]), pk(t[2], t[3]), pk(t[4], t[5]), pk(t[6], t[7])};
    const f32x4 z = {0, 0, 0, 0};
    const f32x4 ax0 = mfma32(as_b8(vx0), as_b8(pb), z), ax1 = mfma32(as_b8(vx1), as_b8(pb), z);
    const f32x4 ax = sel ? ax1 : ax0;
    SCHED();
    DT(4);
    asm volatile("s_waitcnt vmcnt(2)" : "+v"(ua), "+v"(ub)::"memory");
    KEEP(y0); KEEP(y1);
    if (__builtin_expect(any_sent8(ua, ub), 0)) {
      if (TIMING) ++nfm;
      asm volatile("s_waitcnt vmcnt(0)" : "+v"(wa), "+v"(wb)::"memory");
      ua = wa; ub = wb;
      for (unsigned sp = 2; any_sent8(ua, ub); ++sp) {
        asm volatile("" ::: "memory");
        const unsigned r = sp % 3u, so = r == 2u ? (mb ^ 8192u) : mb + r * K_REP;
        ALD16(ua, smv, dsc, so);
        ALD16(ub, smv1, dsc, so);
        asm volatile("s_waitcnt vmcnt(0)" : "+v"(ua), "+v"(ub)::"memory");
        if (sp > SPIN_MAX || tmo) { tmo = 1; break; }
      }
    }
    DT(5);
    const f32x4 sm = __builtin_bit_cast(f32x4, ua) + __builtin_bit_cast(f32x4, ub);
    float m2 = vmax(vmax(sm[0], sm[1]), vmax(sm[2], sm[3]));
    m2 = max16(m2);
    const float M = vmax(m1, m2);
    const float al = __builtin_amdgcn_exp2f(m1 - M);
    const float e0 = __builtin_amdgcn_exp2f(sm[0] - M), e1 = __builtin_amdgcn_exp2f(sm[1] - M), e2 = __builtin_amdgcn_exp2f(sm[2] - M), e3 = __builtin_amdgcn_exp2f(sm[3] - M);
    const u32x2 pm = {pk(e0, e1), pk(e2, e3)};
    const f32x4 cx = ax * al;
    const f32x4 r0 = mfma16(as_s4(vm0), as_s4(pm), cx), r1 = mfma16(as_s4(vm1), as_s4(pm), cx);
    const float l2 = sum16((e0 + e1) + (e2 + e3));
    const float rl = __builtin_amdgcn_rcpf(al * l1 + l2);
    f32x4 r = (sel ? r1 : r0) * rl;
#pragma unroll
    for (int k = 0; k < 4; ++k) {
      const float rk = r[k];
      const int o = __builtin_amdgcn_update_dpp(0, __builtin_bit_cast(int, rk), 0x128, 0xf, 0xf, true);
      r[k] += __builtin_bit_cast(float, o);
    }
    if (sel == 0) *(LAS f32x4*)redw = r;
    BAR();
    DT(6);
    if (wave == 0) {
      float a0[8], a1[8];
#pragma unroll
      for (int w = 0; w < 8; ++w) { a0[w] = redr[w * 128]; a1[w] = redr[w * 128 + 1]; }
      const float s0 = ((a0[0] + a0[1]) + (a0[2] + a0[3])) + ((a0[4] + a0[5]) + (a0[6] + a0[7]));
      const float s1 = ((a1[0] + a1[1]) + (a1[2] + a1[3])) + ((a1[4] + a1[5]) + (a1[6] + a1[7]));
      const unsigned o = san(pk(s0, s1));
      st4(o, rs, mv, mbn);
      st4(o, rs, mv + K_REP, mbn);
      st4(o, rs, mv, mbn ^ 8192u);
      asm volatile("s_waitcnt vmcnt(3)" ::: "memory");
    } else {
      VMWAIT0();
    }
    KEEP(wa); KEEP(wb);
    DT(7);
    DBG_END(s, 7);
  }
  DBG_OUT(P, 32 + cb, wave);
  {
    const int s = NSEG - 1;
    const unsigned mb = mbase(s, pol);
    DELAY(D_MV, 1);
    const u32x4 pend = gather_m(TDP, dsc, gvoff, mb, gdst, lane < 56, tmo, nfail);
    BAR();
    st8(vm0, rs, vmv, (unsigned)(s - 1) * VM_S);
    st8(vm1, rs, vmv + 28 * 512, (unsigned)(s - 1) * VM_S);
    f32x4 c0 = {0, 0, 0, 0}, c1 = {0, 0, 0, 0};
#pragma unroll
    for (int ks = 0; ks < 7; ++ks) {
      const bf16x8 a = *(const LAS bf16x8*)(afp + ks * 1024);
      c0 = smf(a, w0[ks], c0, idxv);
      c1 = smf(a, w1[ks], c1, idxv);
    }
    vm0 = u32x2{pk(c0[0], c0[1]), pk(c0[2], c0[3])};
    vm1 = u32x2{pk(c1[0], c1[1]), pk(c1[2], c1[3])};
    st8(vm0, rs, vmv, (unsigned)s * VM_S);
    st8(vm1, rs, vmv + 28 * 512, (unsigned)s * VM_S);
    VMWAIT0();
    KEEP(pend);
    BAR();
    if (wave == 1 && lane == 0) st4((unsigned)NSEG, rs, chf, 0);
    CNT(P, 1, nfail); CNT(P, 2, nfx); CNT(P, 3, nfm);
    if (cb == 0 && wave == 0) STAMP(P, 4);
  }
}

__device__ __forceinline__ void stage_x(const Params& P, int s, LAS unsigned char* buf, int lane, int first, int count) {
  const unsigned char* p = P.ws + OFF_XF + (size_t)s * XF_S + lane * 16;
  for (int k = first; k < first + count; ++k) glds16(p + k * 1024, buf + k * 1024);
}

template <int MODE>
__device__ __forceinline__ void stream_wg(const Params& P, int id, LAS unsigned char* lds) {
  const int tid = threadIdx.x, lane = tid & 63, wave = __builtin_amdgcn_readfirstlane(tid >> 6);
  const rsrc_t rs = ws_rsrc(P.ws);
  const int n = lane & 15, g = lane >> 4;
  bf16x8 w0[14], w1[14];
  if (MODE == 2) {
#pragma unroll 1
    for (int m = 0; m < 2; ++m) {
      const unsigned short* W = m ? P.Wu : P.Wg;
      for (int k = wave; k < 98; k += 8) {
        const int i = k * 64 + lane, d = i / 14, ch = i - d * 14;
        glds16(W + (size_t)d * 1792 + 112 * id + 8 * ch, lds + k * 1024);
      }
      VMWAIT0();
      __syncthreads();
      if (wave < 7) {
        const LAS unsigned char* tb = lds + (8 * g + (n >> 2)) * 224 + (n & 3) * 8 + wave * 32;
#pragma unroll
        for (int kk = 0; kk < 14; ++kk) {
          typedef short v4s __attribute__((ext_vector_type(4)));
          const v4s lo = __builtin_amdgcn_ds_read_tr16_b64_v4i16((LAS v4s*)(tb + kk * 32 * 224));
          const v4s hi = __builtin_amdgcn_ds_read_tr16_b64_v4i16((LAS v4s*)(tb + kk * 32 * 224 + 4 * 224));
          const bf16x8 f = __builtin_bit_cast(bf16x8, __builtin_shufflevector(lo, hi, 0, 1, 2, 3, 4, 5, 6, 7));
          if (m == 0) w0[kk] = f; else w1[kk] = f;
        }
      }
      __syncthreads();
    }
  } else if (wave < 7) {
    const int h = id >> 1, hq = id & 1;
    const unsigned char* b0 = P.ws + (MODE ? OFF_WV : OFF_WK) + ((size_t)(h * 28 + 14 * hq + 2 * wave) * 14) * 1024 + lane * 16;
#pragma unroll
    for (int kk = 0; kk < 14; ++kk) { w0[kk] = *(const bf16x8*)(b0 + kk * 1024); w1[kk] = *(const bf16x8*)(b0 + (14 + kk) * 1024); }
  }
  const unsigned fl = (MODE == 0 ? FL_T1K : MODE == 1 ? FL_T1V : FL_F1) + (unsigned)id * FS;
  if (wave == 7) {
    stage_x(P, 0, lds, lane, 0, 28);
    stage_x(P, 1, lds + 28672, lane, 0, 28);
    asm volatile("s_waitcnt vmcnt(28)" ::: "memory");
    int nb = 2;
    for (int s = 0; s < NSEG; ++s) {
      BAR();
      if (id == 0) STAMP(P, 192 + MODE * 64 + s);
      if (s + 2 < NSEG) { stage_x(P, s + 2, lds + nb * 28672, lane, 0, 28); asm volatile("s_waitcnt vmcnt(28)" ::: "memory"); }
      else VMWAIT0();
      nb = nb == 2 ? 0 : nb + 1;
    }
    BAR();
    if (lane == 0) st4((unsigned)NSEG, rs, fl, 0);
  } else {
    int cbuf = 0;
    for (int s = 0; s < NSEG; ++s) {
      BAR();
      if (wave == 0 && s > 0 && lane == 0) st4((unsigned)(s <= 2 ? s : s - 1), rs, fl, 0);
      const LAS unsigned char* xb = lds + cbuf * 28672 + lane * 16;
      cbuf = cbuf == 2 ? 0 : cbuf + 1;
      f32x4 c00 = {0, 0, 0, 0}, c01 = {0, 0, 0, 0}, c10 = {0, 0, 0, 0}, c11 = {0, 0, 0, 0};
      bf16x8 xa[3], xc[3];
      xa[0] = *(const LAS bf16x8*)(xb); xc[0] = *(const LAS bf16x8*)(xb + 14 * 1024);
      xa[1] = *(const LAS bf16x8*)(xb + 1024); xc[1] = *(const LAS bf16x8*)(xb + 15 * 1024);
#pragma unroll
      for (int kk = 0; kk < 14; ++kk) {
        if (kk + 2 < 14) { xa[(kk + 2) % 3] = *(const LAS bf16x8*)(xb + (kk + 2) * 1024); xc[(kk + 2) % 3] = *(const LAS bf16x8*)(xb + (16 + kk) * 1024); }
        SCHED();
        const bf16x8 x0 = xa[kk % 3], x1 = xc[kk % 3];
        if (MODE == 1) { c00 = mfma32(x0, w0[kk], c00); c01 = mfma32(x0, w1[kk], c01); c10 = mfma32(x1, w0[kk], c10); c11 = mfma32(x1, w1[kk], c11); }
        else { c00 = mfma32(w0[kk], x0, c00); c01 = mfma32(w1[kk], x0, c01); c10 = mfma32(w0[kk], x1, c10); c11 = mfma32(w1[kk], x1, c11); }
        SCHED();
      }
      if (MODE == 2) {
#pragma unroll
        for (int q = 0; q < 2; ++q) {
          const f32x4 cg = q ? c10 : c00, cu = q ? c11 : c01;
          float hv[4];
#pragma unroll
          for (int k = 0; k < 4; ++k) {
            const float gg = cg[k];
            hv[k] = gg * __builtin_amdgcn_rcpf(1.0f + __builtin_amdgcn_exp2f(-1.4426950408889634f * gg)) * cu[k];
          }
          const u32x2 o = {pk(hv[0], hv[1]), pk(hv[2], hv[3])};
          st8(o, rs, OFF_HF + (unsigned)(q * 112 + 7 * id + wave) * 512u + lane * 8, (unsigned)s * HF_S);
        }
      } else if (MODE == 0) {
        const int h = id >> 1, Pp = 7 * (id & 1) + wave;
        const u32x4 o0 = {pk(c00[0], c00[1]), pk(c00[2], c00[3]), pk(c01[0], c01[1]), pk(c01[2], c01[3])};
        const u32x4 o1 = {pk(c10[0], c10[1]), pk(c10[2], c10[3]), pk(c11[0], c11[1]), pk(c11[2], c11[3])};
        st16(o0, rs, OFF_KX + (unsigned)((h * 2 + 0) * 14 + Pp) * 1024u + lane * 16, (unsigned)s * KX_S);
        st16(o1, rs, OFF_KX + (unsigned)((h * 2 + 1) * 14 + Pp) * 1024u + lane * 16, (unsigned)s * KX_S);
      } else {
        const int h = id >> 1, cb = 14 * (id & 1) + 2 * wave;
        const u32x4 o0 = {pk(c00[0], c00[1]), pk(c00[2], c00[3]), pk(c10[0], c10[1]), pk(c10[2], c10[3])};
        const u32x4 o1 = {pk(c01[0], c01[1]), pk(c01[2], c01[3]), pk(c11[0], c11[1]), pk(c11[2], c11[3])};
        st16(o0, rs, OFF_VX + (unsigned)(h * 28 + cb) * 1024u + lane * 16, (unsigned)s * VX_S);
        st16(o1, rs, OFF_VX + (unsigned)(h * 28 + cb + 1) * 1024u + lane * 16, (unsigned)s * VX_S);
      }
      if (s < 2) VMWAIT0(); else asm volatile("s_waitcnt vmcnt(2)" ::: "memory");
    }
    VMWAIT0();
    BAR();
  }
}

template <int SLEEP>
__device__ __forceinline__ void wait_lanes(rsrc_t rs, unsigned voff, int need, int& tmo) {
  for (unsigned sp = 0;; ++sp) {
    asm volatile("" ::: "memory");
    const int v = (int)ld4(rs, voff, 0);
    if (__builtin_amdgcn_ballot_w64(v < need) == 0ull) break;
    if (sp > SPIN_MAX || tmo) { tmo = 1; break; }
    __builtin_amdgcn_s_sleep(SLEEP);
  }
}

__device__ __forceinline__ void task_sxx(const Params& P, int s, LAS unsigned char* lds, int& tmo) {
  int tid = threadIdx.x;
  asm volatile("" : "+v"(tid));
  const int lane = tid & 63, wave = __builtin_amdgcn_readfirstlane(tid >> 6);
  const rsrc_t rs = ws_rsrc(P.ws);
  __syncthreads();
  stage_x(P, s, lds, lane, wave * 4, wave < 7 ? 4 : 0);
  if (wave == 0) wait_lanes<24>(rs, FL_T1K + (unsigned)(lane & 31) * FS, s + 1, tmo);
  BAR();
  const unsigned kv = lane * 16;
  const unsigned so = OFF_KX + (unsigned)(2 * wave * 2 * 14) * 1024u + (unsigned)s * KX_S;
  u32x4 ka[2][7], kb[2][7];
#pragma unroll
  for (int k = 0; k < 7; ++k) { ka[0][k] = ld16(rs, kv, so + k * 1024); kb[0][k] = ld16(rs, kv, so + (14 + k) * 1024); }
  asm volatile("s_waitcnt vmcnt(14)" ::: "memory");
  __syncthreads();
  const LAS unsigned char* xb = lds + lane * 16;
  f32x4 c00 = {0, 0, 0, 0}, c01 = {0, 0, 0, 0}, c11 = {0, 0, 0, 0};
#pragma unroll
  for (int c = 0; c < 4; ++c) {
    if (c + 1 < 4) {
      const int hn = (c + 1) >> 1, kn = ((c + 1) & 1) * 7;
#pragma unroll
      for (int k = 0; k < 7; ++k) { ka[(c + 1) & 1][k] = ld16(rs, kv, so + (hn * 28 + kn + k) * 1024); kb[(c + 1) & 1][k] = ld16(rs, kv, so + (hn * 28 + 14 + kn + k) * 1024); }
    }
    SCHED();
#pragma unroll
    for (int k = 0; k < 7; ++k) {
      const int kk = (c & 1) * 7 + k;
      const bf16x8 x0 = *(const LAS bf16x8*)(xb + kk * 1024), x1 = *(const LAS bf16x8*)(xb + (14 + kk) * 1024);
      c00 = mfma32(as_b8(ka[c & 1][k]), x0, c00);
      c01 = mfma32(as_b8(ka[c & 1][k]), x1, c01);
      c11 = mfma32(as_b8(kb[c & 1][k]), x1, c11);
    }
    if (c & 1) {
      const unsigned o = OFF_SXX + (unsigned)((2 * wave + (c >> 1)) * 3) * 1024u + (unsigned)s * SXX_S;
      st16(__builtin_bit_cast(u32x4, c00), rs, kv, o);
      st16(__builtin_bit_cast(u32x4, c01), rs, kv, o + 1024);
      st16(__builtin_bit_cast(u32x4, c11), rs, kv, o + 2048);
      c00 = f32x4{0, 0, 0, 0}; c01 = f32x4{0, 0, 0, 0}; c11 = f32x4{0, 0, 0, 0};
    }
    SCHED();
  }
  VMWAIT0();
  __syncthreads();
  if (tid == 0) st4(1u, rs, FL_T2 + (unsigned)s * FS, 0);
}

__device__ __forceinline__ void task_ffn2(const Params& P, int sp, LAS unsigned char* lds, int& tmo) {
  int tid = threadIdx.x;
  asm volatile("" : "+v"(tid));
  const int lane = tid & 63, wave = __builtin_amdgcn_readfirstlane(tid >> 6);
  const rsrc_t rs = ws_rsrc(P.ws);
  __syncthreads();
  const unsigned char* wd = P.ws + OFF_WD + (size_t)(4 * (wave < 7 ? wave : 0)) * 56 * 1024 + lane * 16;
  bf16x8 wf[4][4];
#pragma unroll
  for (int uu = 0; uu < 4; ++uu)
#pragma unroll
    for (int c = 0; c < 4; ++c) wf[uu][c] = *(const bf16x8*)(wd + (size_t)(c * 56 + uu) * 1024);
  if (wave == 0) wait_lanes<24>(rs, FL_F1 + (unsigned)(lane & 15) * FS, 2 * sp + 2, tmo);
  BAR();
  f32x4 acc[4][4];
#pragma unroll
  for (int c = 0; c < 4; ++c)
#pragma unroll
    for (int q = 0; q < 4; ++q) acc[c][q] = f32x4{0, 0, 0, 0};
#pragma unroll 1
  for (int half = 0; half < 2; ++half) {
    if (half) __syncthreads();
    {
      u32x4 v[14];
#pragma unroll
      for (int k = 0; k < 14; ++k) {
        const int pc = wave * 14 + k, tile = pc / 28, f2 = pc - tile * 28;
        v[k] = ld16(rs, lane * 16, OFF_HF + (unsigned)(2 * sp + (tile >> 1)) * HF_S + (unsigned)((tile & 1) * 112 + 56 * half + 2 * f2) * 512u);
      }
#pragma unroll
      for (int k = 0; k < 14; ++k) *(LAS u32x4*)(lds + (wave * 14 + k) * 1024 + lane * 16) = v[k];
    }
    __syncthreads();
    if (wave < 7) {
      const LAS unsigned char* hb = lds + lane * 8;
#pragma unroll 1
      for (int u0 = 0; u0 < 28; u0 += 4) {
        const int ug = 28 * half + u0;
        const int un = ug + 4 < 56 ? ug + 4 : ug;
#pragma unroll
        for (int uu = 0; uu < 4; ++uu) {
          const int u = u0 + uu;
          u32x4 hf[4];
#pragma unroll
          for (int q = 0; q < 4; ++q) {
            const u32x2 lo = *(const LAS u32x2*)(hb + (q * 56 + 2 * u) * 512), hi = *(const LAS u32x2*)(hb + (q * 56 + 2 * u + 1) * 512);
            hf[q] = u32x4{lo.x, lo.y, hi.x, hi.y};
          }
#pragma unroll
          for (int q = 0; q < 4; ++q)
#pragma unroll
            for (int c = 0; c < 4; ++c) acc[c][q] = mfma32(wf[uu][c], as_b8(hf[q]), acc[c][q]);
          SCHED();
#pragma unroll
          for (int c = 0; c < 4; ++c) wf[uu][c] = *(const bf16x8*)(wd + (size_t)(c * 56 + un + uu) * 1024);
          SCHED();
        }
      }
    } else if (half == 0) {
      const unsigned short* xp = P.x + (size_t)(64 * sp + lane) * DM;
      float acc1 = 0.f;
#pragma unroll 7
      for (int k = 0; k < 56; ++k) {
        const u32x4 xv = *(const u32x4*)(xp + 8 * k), gv = *(const u32x4*)(P.gw + 8 * k);
        acc1 += bflo(xv.x) * bflo(gv.x) + bfhi(xv.x) * bfhi(gv.x) + bflo(xv.y) * bflo(gv.y) + bfhi(xv.y) * bfhi(gv.y) +
                bflo(xv.z) * bflo(gv.z) + bfhi(xv.z) * bfhi(gv.z) + bflo(xv.w) * bflo(gv.w) + bfhi(xv.w) * bfhi(gv.w);
      }
      const float gate = __builtin_amdgcn_rcpf(1.0f + __builtin_amdgcn_exp2f(-1.4426950408889634f * acc1));
      st4(__builtin_bit_cast(unsigned, gate), rs, (unsigned)lane * 4u, OFF_G + (unsigned)(64 * sp) * 4u);
    }
  }
  if (wave < 7) {
#pragma unroll
    for (int c = 0; c < 4; ++c)
#pragma unroll
      for (int q = 0; q < 4; ++q) {
        const u32x2 o = {pk(acc[c][q][0], acc[c][q][1]), pk(acc[c][q][2], acc[c][q][3])};
        st8(o, rs, lane * 8, OFF_BF + (unsigned)((q & 1) * 28 + 4 * wave + c) * 512u + (unsigned)(2 * sp + (q >> 1)) * BF_S);
      }
  }
  VMWAIT0();
  __syncthreads();
  if (tid == 0) st4(1u, rs, FL_F2 + (unsigned)sp * FS, 0);
}

template <int Q>
__device__ __forceinline__ void attn_softmax(const f32x4 sm, const f32x4 ta, const f32x4 tb, int g, int n, u32x4& pt, u32x2& pm) {
  const float NEG = -3.0e38f;
  float t[8];
#pragma unroll
  for (int k = 0; k < 4; ++k) {
    const bool causal = (4 * g + k) <= n;
    t[k] = Q ? ta[k] : (causal ? ta[k] : NEG);
    t[4 + k] = Q ? (causal ? tb[k] : NEG) : NEG;
  }
  float m = vmax(vmax(vmax(sm[0], sm[1]), vmax(sm[2], sm[3])), vmax(vmax(t[0], t[1]), vmax(t[2], t[3])));
  if (Q) m = vmax(m, vmax(vmax(t[4], t[5]), vmax(t[6], t[7])));
  m = max32(max16(m));
  float e[4], l = 0.f;
#pragma unroll
  for (int k = 0; k < 4; ++k) { e[k] = __builtin_amdgcn_exp2f(sm[k] - m); l += e[k]; }
  l *= 0.5f;
#pragma unroll
  for (int k = 0; k < (Q ? 8 : 4); ++k) { t[k] = __builtin_amdgcn_exp2f(t[k] - m); l += t[k]; }
  l = sum32(sum16(l));
  const float rl = __builtin_amdgcn_rcpf(l);
  pt = u32x4{pk(t[0] * rl, t[1] * rl), pk(t[2] * rl, t[3] * rl), Q ? pk(t[4] * rl, t[5] * rl) : 0u, Q ? pk(t[6] * rl, t[7] * rl) : 0u};
  pm = u32x2{pk(e[0] * rl, e[1] * rl), pk(e[2] * rl, e[3] * rl)};
}

__device__ __forceinline__ void task_attn(const Params& P, int s, int cbg, LAS unsigned char* lds, int& tmo) {
  int tid = threadIdx.x;
  asm volatile("" : "+v"(tid));
  const int lane = tid & 63, wave = __builtin_amdgcn_readfirstlane(tid >> 6);
  const rsrc_t rs = ws_rsrc(P.ws);
  const int n = lane & 15, g = lane >> 4;
  LAS unsigned char* PT = lds + 8192;
  LAS unsigned char* PM = PT + 32768;
  LAS unsigned char* VS = PM + 16384;
  __syncthreads();
  {
    const unsigned fo = lane < 32 ? FL_T1V + (unsigned)lane * FS : (lane & 1) ? FL_T2 + (unsigned)s * FS : FL_F2 + (unsigned)(s >> 1) * FS;
    if (wave == 0) wait_lanes<24>(rs, fo, lane < 32 ? s + 1 : 1, tmo);
    BAR();
  }
  const int cbl = wave >> 1, hh8 = wave & 1, cb = 4 * cbg + cbl, qo = hh8;
  f32x4 sxx[2][3];
  u32x2 bf;
  float gate;
  {
    u32x4 vx[8];
#pragma unroll
    for (int k = 0; k < 8; ++k) vx[k] = ld16(rs, lane * 16, OFF_VX + (unsigned)((2 * wave + (k >> 2)) * 28 + 4 * cbg + (k & 3)) * 1024u + (unsigned)s * VX_S);
#pragma unroll
    for (int hh = 0; hh < 2; ++hh)
#pragma unroll
      for (int k = 0; k < 3; ++k) sxx[hh][k] = __builtin_bit_cast(f32x4, ld16(rs, lane * 16, OFF_SXX + (unsigned)((2 * wave + hh) * 3 + k) * 1024u + (unsigned)s * SXX_S));
    bf = ld8(rs, lane * 8, OFF_BF + (unsigned)(qo * 28 + cb) * 512u + (unsigned)s * BF_S);
    gate = __builtin_bit_cast(float, ld4(rs, (unsigned)n * 4u, OFF_G + (unsigned)(32 * s + 16 * qo) * 4u));
#pragma unroll
    for (int k = 0; k < 8; ++k) *(LAS u32x4*)(VS + ((2 * wave + (k >> 2)) * 4 + (k & 3)) * 1024 + lane * 16) = vx[k];
  }
  VMWAIT0();
  __syncthreads();
  if (s == NSEG - 1 && cbg == 0 && wave == 0) STAMP(P, 15);
  if (wave == 0) {
    const unsigned vo = FL_CH + (unsigned)(lane < 60 ? lane : 0) * FS;
    for (unsigned sp = 0;; ++sp) {
      asm volatile("" ::: "memory");
      const int v = (int)ld4(rs, vo, 0);
      if (__builtin_amdgcn_ballot_w64(v < s + 1) == 0ull) break;
      if (sp > SPIN_MAX || tmo) { tmo = 1; break; }
      const int v0 = __builtin_amdgcn_readfirstlane(v);
      const int far = min(s - v0 - 1, 16);
      for (int k = 0; k < far; ++k) __builtin_amdgcn_s_sleep(48);
      __builtin_amdgcn_s_sleep(4);
    }
  }
  BAR();
  if (s == NSEG - 1 && cbg == 0 && wave == 0) STAMP(P, 16);
  f32x4 smq[2][2];
  u32x2 vm[8];
  {
    u32x4 sp[2][2][2];
#pragma unroll
    for (int hh = 0; hh < 2; ++hh)
#pragma unroll
      for (int q = 0; q < 2; ++q)
#pragma unroll
        for (int hq = 0; hq < 2; ++hq)
          sp[hh][q][hq] = ld16(rs, (unsigned)((g & 1) * 16 + n) * 16u, OFF_KM + (unsigned)((2 * wave + hh) * 2 + hq) * 1024u + (unsigned)q * 512u + (unsigned)s * KM_S);
#pragma unroll
    for (int h = 0; h < 8; ++h) vm[h] = ld8(rs, lane * 8, OFF_VM + (unsigned)((8 * hh8 + h) * 28 + cb) * 512u + (unsigned)s * VM_S);
    SCHED();
#pragma unroll
    for (int hh = 0; hh < 2; ++hh)
#pragma unroll
      for (int q = 0; q < 2; ++q) smq[hh][q] = __builtin_bit_cast(f32x4, sp[hh][q][0]) + __builtin_bit_cast(f32x4, sp[hh][q][1]);
  }
  if (s == NSEG - 1 && cbg == 0 && wave == 0) STAMP(P, 17);
#pragma unroll
  for (int hh = 0; hh < 2; ++hh) {
    const int h = 2 * wave + hh;
    u32x4 pt;
    u32x2 pm;
    attn_softmax<0>(smq[hh][0], sxx[hh][0], sxx[hh][2], g, n, pt, pm);
    *(LAS u32x4*)(PT + (h * 2 + 0) * 1024 + lane * 16) = pt;
    *(LAS u32x2*)(PM + (h * 2 + 0) * 512 + lane * 8) = pm;
    attn_softmax<1>(smq[hh][1], sxx[hh][1], sxx[hh][2], g, n, pt, pm);
    *(LAS u32x4*)(PT + (h * 2 + 1) * 1024 + lane * 16) = pt;
    *(LAS u32x2*)(PM + (h * 2 + 1) * 512 + lane * 8) = pm;
  }
  __syncthreads();
  if (s == NSEG - 1 && cbg == 0 && wave == 0) STAMP(P, 18);
  f32x4 at[2] = {{0, 0, 0, 0}, {0, 0, 0, 0}}, am[2] = {{0, 0, 0, 0}, {0, 0, 0, 0}};
#pragma unroll
  for (int k = 0; k < 8; ++k) {
    const int h = 8 * hh8 + k;
    const bf16x8 vx = *(const LAS bf16x8*)(VS + (h * 4 + cbl) * 1024 + lane * 16);
#pragma unroll
    for (int q = 0; q < 2; ++q) {
      const bf16x8 pt = *(const LAS bf16x8*)(PT + (h * 2 + q) * 1024 + lane * 16);
      const u32x2 pm = *(const LAS u32x2*)(PM + (h * 2 + q) * 512 + lane * 8);
      at[q] = mfma32(vx, pt, at[q]);
      am[q] = mfma16(as_s4(vm[k]), as_s4(pm), am[q]);
    }
  }
  LAS f32x4* ex = (LAS f32x4*)lds;
  ex[(wave ^ 1) * 64 + lane] = hh8 ? at[0] + am[0] : at[1] + am[1];
  __syncthreads();
  const f32x4 acc = (hh8 ? at[1] + am[1] : at[0] + am[0]) + ex[wave * 64 + lane];
  const float gb = bflo(pk(gate, 0.f));
  const float og = bflo(pk(1.0f - gb, 0.f));
  const float bv[4] = {bflo(bf.x), bfhi(bf.x), bflo(bf.y), bfhi(bf.y)};
  float o[4];
#pragma unroll
  for (int k = 0; k < 4; ++k) {
    const float av = bflo(pk(acc[k], 0.f));
    o[k] = bflo(pk(gb * av, 0.f)) + bflo(pk(og * bv[k], 0.f));
  }
  const u32x2 ov = {pk(o[0], o[1]), pk(o[2], o[3])};
  *(u32x2*)(P.out + (size_t)(32 * s + 16 * qo + n) * DM + 16 * cb + 4 * g) = ov;
}

constexpr int NTASK = 32 * 17;
__device__ __forceinline__ void generic_wg(const Params& P, LAS unsigned char* lds) {
  int tmo = 0;
  LAS int* tk = (LAS int*)(lds + LDS_BYTES - 16);
  for (;;) {
    __syncthreads();
    if (threadIdx.x == 0) *tk = (int)__hip_atomic_fetch_add((GAS unsigned*)(P.ws + FL_Q), 1u, __ATOMIC_RELAXED, __HIP_MEMORY_SCOPE_AGENT);
    __syncthreads();
    const int t = __builtin_amdgcn_readfirstlane(*tk);
    if (t >= NTASK) { if (threadIdx.x < 64) LSTAMP(P, 768 + blockIdx.x); break; }
    int kind = 2, s = 0, c = 0;
    {
      int cnt = t, npre = 0, p = -1;
      for (;;) {
        const int lim = min(32, p < 0 ? 2 : (3 * (p + 1)) / 2 + 2);
        if (cnt < 3 * (lim - npre)) { const int pp = npre + cnt / 3, o = cnt % 3; kind = o == 0 ? 1 : 0; s = o == 0 ? pp : 2 * pp + o - 1; break; }
        cnt -= 3 * (lim - npre); npre = lim; ++p;
        if (cnt < 14) { kind = 2; s = 2 * p + (cnt >= 7 ? 1 : 0); c = cnt >= 7 ? cnt - 7 : cnt; break; }
        cnt -= 14;
      }
    }
    if (threadIdx.x < 64) STAMP(P, 1024 + 2 * t);
    if (kind == 0) task_sxx(P, s, lds, tmo);
    else if (kind == 1) task_ffn2(P, s, lds, tmo);
    else task_attn(P, s, c, lds, tmo);
    if (threadIdx.x < 64) STAMP(P, 1024 + 2 * t + 1);
  }
}

__global__ void __launch_bounds__(512, 2) main_kernel(Params P) {
  extern __shared__ __attribute__((aligned(16))) unsigned char smem[];
  LAS unsigned char* lds = (LAS unsigned char*)smem;
  const int b = blockIdx.x, r = b >> 3;
  const int x = (b + (int)*(const unsigned*)(P.ws + FL_ROT)) & 7;
  if (threadIdx.x < 64) { STAMP(P, 512 + b); LSTAMP(P, 512 + b); }
  if (x < 4) {
    if (x == 0 || x == 2) {
      if (r < 16) {
        const int idx = (x >> 1) * 16 + r;
        const int wave = __builtin_amdgcn_readfirstlane(threadIdx.x >> 6);
        if (wave < 7) score_main(P, idx, wave, lds); else score_sx(P, idx, lds);
      }
      else if (x == 0 && r == 16) probe_pol(P);
    } else if (r < 14) {
      value_wg(P, (x >> 1) * 14 + r, lds);
    }
  } else {
    const int y = x - 4;
    if (r < 8) stream_wg<0>(P, y * 8 + r, lds);
    else if (r < 16) stream_wg<1>(P, y * 8 + r - 8, lds);
    else if (r < 20) stream_wg<2>(P, y * 4 + r - 16, lds);
    generic_wg(P, lds);
  }
}

}

extern "C" void kernel_launch(void* const* d_in, const int* in_sizes, int n_in, void* d_out, int out_size, void* d_ws, size_t ws_size,
                              hipStream_t stream) {
  Params P;
  P.x = (const unsigned short*)d_in[0];
  P.mem0 = (const unsigned short*)d_in[1];
  P.Wq = (const unsigned short*)d_in[2];
  P.Wk = (const unsigned short*)d_in[3];
  P.Wv = (const unsigned short*)d_in[4];
  P.Wo = (const unsigned short*)d_in[5];
  P.Wg = (const unsigned short*)d_in[8];
  P.Wu = (const unsigned short*)d_in[9];
  P.Wd = (const unsigned short*)d_in[10];
  P.gw = (const unsigned short*)d_in[11];
  P.out = (unsigned short*)d_out;
  const uintptr_t base = ((uintptr_t)d_ws + ((1u << 21) - 1)) & ~(uintptr_t)((1u << 21) - 1);
  P.ws = (unsigned char*)base;
  (void)hipFuncSetAttribute((const void*)prep_kernel, hipFuncAttributeMaxDynamicSharedMemorySize, 57344);
  (void)hipFuncSetAttribute((const void*)main_kernel, hipFuncAttributeMaxDynamicSharedMemorySize, LDS_BYTES);
  hipLaunchKernelGGL(prep_kernel, dim3(256), dim3(512), 57344, stream, P);
  hipLaunchKernelGGL(main_kernel, dim3(256), dim3(512), LDS_BYTES, stream, P);
}
```

```cpp
#include <hip/hip_runtime.h>
#include <stdint.h>

namespace {

typedef __bf16 bf16x2 __attribute__((ext_vector_type(2)));
typedef __bf16 bf16x8 __attribute__((ext_vector_type(8)));
typedef __bf16 bf16x16 __attribute__((ext_vector_type(16)));
typedef short s16x4 __attribute__((ext_vector_type(4)));
typedef float f32x4 __attribute__((ext_vector_type(4)));
typedef unsigned u32x4 __attribute__((ext_vector_type(4)));
typedef unsigned u32x2 __attribute__((ext_vector_type(2)));
#define LAS __attribute__((address_space(3)))
#define GAS __attribute__((address_space(1)))
typedef __amdgpu_buffer_rsrc_t rsrc_t;

constexpr unsigned SENT = 0xFFFFFFFFu;
constexpr int NSEG = 64;
constexpr int DM = 448;
constexpr int AUX_SC1 = 16;

constexpr unsigned SZ_W = 16u * 28 * 14 * 1024;
constexpr unsigned OFF_WK = 0;
constexpr unsigned OFF_WV = OFF_WK + SZ_W;
constexpr unsigned OFF_KX = OFF_WV + SZ_W;
constexpr unsigned KX_S = 16u * 2 * 14 * 1024;
constexpr unsigned OFF_VX = OFF_KX + 64u * KX_S;
constexpr unsigned VX_S = 16u * 28 * 1024;
constexpr unsigned OFF_KM = OFF_VX + 64u * VX_S;
constexpr unsigned KM_S = 16u * 14 * 512;
constexpr unsigned OFF_VM = OFF_KM + 64u * KM_S;
constexpr unsigned VM_S = 16u * 28 * 512;
constexpr unsigned OFF_WD = OFF_VM + 64u * VM_S;
constexpr unsigned OFF_HF = OFF_WD + 28u * 56 * 1024;
constexpr unsigned HF_S = 2u * 112 * 512;
constexpr unsigned OFF_BF = OFF_HF + 64u * HF_S;
constexpr unsigned BF_S = 2u * 28 * 512;
constexpr unsigned OFF_SXX = OFF_BF + 64u * BF_S;
constexpr unsigned SXX_S = 16u * 3 * 1024;
constexpr unsigned OFF_XF = OFF_SXX + 64u * SXX_S;
constexpr unsigned XF_S = 28u * 1024;
constexpr unsigned OFF_G = OFF_XF + 64u * XF_S;
constexpr unsigned OFF_FL = OFF_G + 8192;
constexpr unsigned FS = 128;
constexpr unsigned FL_T1K = OFF_FL;
constexpr unsigned FL_T1V = FL_T1K + 32 * FS;
constexpr unsigned FL_F1 = FL_T1V + 32 * FS;
constexpr unsigned FL_CH = FL_F1 + 16 * FS;
constexpr unsigned FL_T2 = FL_CH + 64 * FS;
constexpr unsigned FL_F2 = FL_T2 + 64 * FS;
constexpr unsigned FL_POL = FL_F2 + 64 * FS;
constexpr unsigned FL_ROT = FL_POL + FS;
constexpr unsigned FL_Q = FL_POL - FS;
constexpr unsigned FL_COUNT = 32 + 32 + 16 + 64 + 64 + 64 + 1;
constexpr unsigned OFF_MB = 112u << 20;
constexpr unsigned WS_USED = OFF_MB + (8u << 20);
static_assert(FL_ROT + FS <= OFF_MB, "workspace map");
constexpr unsigned K_SM = 0, K_SX = 32768, K_M = 65536, K_REP = 16384, K_PROBE = 98304;

constexpr unsigned SPIN_MAX = 40000u;
constexpr int LDS_BYTES = 144 * 1024;

struct Params {
  const unsigned short* x;
  const unsigned short* mem0;
  const unsigned short* Wq;
  const unsigned short* Wk;
  const unsigned short* Wv;
  const unsigned short* Wo;
  const unsigned short* Wg;
  const unsigned short* Wu;
  const unsigned short* Wd;
  const unsigned short* gw;
  unsigned short* out;
  unsigned char* ws;
};

constexpr int D_MS = 30;
constexpr int D_MV = 8;
constexpr int D_MV0 = 4;
constexpr int D_GAP = 5;
constexpr int D_SX = 13;
constexpr int D_G2 = 0;
constexpr int D_SM = 2;
constexpr int D_BULK = 4;
constexpr int NOLAG = 2;

__device__ __forceinline__ unsigned pk(float a, float b) {
  bf16x2 v = {(__bf16)a, (__bf16)b};
  return __builtin_bit_cast(unsigned, v);
}
__device__ __forceinline__ unsigned san(unsigned v) { return min(v, 0xFFFFFFFEu); }
__device__ __forceinline__ float bflo(unsigned v) { return __builtin_bit_cast(float, v << 16); }
__device__ __forceinline__ float bfhi(unsigned v) { return __builtin_bit_cast(float, v & 0xFFFF0000u); }
__device__ __forceinline__ float vmax(float a, float b) { return __builtin_elementwise_maximum(a, b); }
template <int N> __device__ __forceinline__ void delay_c() {
#pragma unroll 1
  for (int i = 0; i < N; ++i) __builtin_amdgcn_s_sleep(1);
}
__device__ __forceinline__ float max16(float v) {
  unsigned u = __builtin_bit_cast(unsigned, v);
  auto rr = __builtin_amdgcn_permlane16_swap(u, u, false, false);
  const unsigned r0 = rr[0], r1 = rr[1];
  return vmax(__builtin_bit_cast(float, r0), __builtin_bit_cast(float, r1));
}
__device__ __forceinline__ float max32(float v) {
  unsigned u = __builtin_bit_cast(unsigned, v);
  auto rr = __builtin_amdgcn_permlane32_swap(u, u, false, false);
  const unsigned r0 = rr[0], r1 = rr[1];
  return vmax(__builtin_bit_cast(float, r0), __builtin_bit_cast(float, r1));
}
__device__ __forceinline__ float sum16(float v) {
  unsigned u = __builtin_bit_cast(unsigned, v);
  auto rr = __builtin_amdgcn_permlane16_swap(u, u, false, false);
  const unsigned r0 = rr[0], r1 = rr[1];
  return __builtin_bit_cast(float, r0) + __builtin_bit_cast(float, r1);
}
__device__ __forceinline__ float sum32(float v) {
  unsigned u = __builtin_bit_cast(unsigned, v);
  auto rr = __builtin_amdgcn_permlane32_swap(u, u, false, false);
  const unsigned r0 = rr[0], r1 = rr[1];
  return __builtin_bit_cast(float, r0) + __builtin_bit_cast(float, r1);
}
__device__ __forceinline__ bf16x8 as_b8(u32x4 v) { return __builtin_bit_cast(bf16x8, v); }
__device__ __forceinline__ s16x4 as_s4(u32x2 v) { return __builtin_bit_cast(s16x4, v); }
__device__ __forceinline__ bf16x16 cat16(bf16x8 a, bf16x8 b) {
  return __builtin_shufflevector(a, b, 0, 1, 2, 3, 4, 5, 6, 7, 8, 9, 10, 11, 12, 13, 14, 15);
}
__device__ __forceinline__ f32x4 mfma32(bf16x8 a, bf16x8 b, f32x4 c) { return __builtin_amdgcn_mfma_f32_16x16x32_bf16(a, b, c, 0, 0, 0); }
__device__ __forceinline__ f32x4 mfma16(s16x4 a, s16x4 b, f32x4 c) { return __builtin_amdgcn_mfma_f32_16x16x16bf16_1k(a, b, c, 0, 0, 0); }
__device__ __forceinline__ f32x4 smf(bf16x8 a, bf16x16 b, f32x4 c, int idx) { return __builtin_amdgcn_smfmac_f32_16x16x64_bf16(a, b, c, idx, 0, 0); }
__device__ __forceinline__ u32x4 ld16(rsrc_t rs, unsigned v, unsigned s) { return __builtin_amdgcn_raw_buffer_load_b128(rs, v, s, AUX_SC1); }
__device__ __forceinline__ u32x2 ld8(rsrc_t rs, unsigned v, unsigned s) { return __builtin_amdgcn_raw_buffer_load_b64(rs, v, s, AUX_SC1); }
__device__ __forceinline__ unsigned ld4(rsrc_t rs, unsigned v, unsigned s) { return __builtin_amdgcn_raw_buffer_load_b32(rs, v, s, AUX_SC1); }
__device__ __forceinline__ void st16(u32x4 d, rsrc_t rs, unsigned v, unsigned s) {
  __builtin_amdgcn_raw_buffer_store_b128(d, rs, v, s, AUX_SC1);
  asm volatile("s_nop 1" : "+v"(d)::"memory");
}
__device__ __forceinline__ void st8(u32x2 d, rsrc_t rs, unsigned v, unsigned s) { __builtin_amdgcn_raw_buffer_store_b64(d, rs, v, s, AUX_SC1); }
__device__ __forceinline__ void st4(unsigned d, rsrc_t rs, unsigned v, unsigned s) { __builtin_amdgcn_raw_buffer_store_b32(d, rs, v, s, AUX_SC1); }
__device__ __forceinline__ rsrc_t ws_rsrc(unsigned char* ws) { return __builtin_amdgcn_make_buffer_rsrc(ws, 0, WS_USED, 0x00020000); }
__device__ __forceinline__ void glds16(const void* src, LAS unsigned char* dst) {
  __builtin_amdgcn_global_load_lds((const GAS unsigned*)src, (LAS unsigned*)dst, 16, 0, 0);
}
__device__ __forceinline__ u32x4 ws_desc(unsigned char* ws) {
  const unsigned long long a = (unsigned long long)ws;
  return u32x4{(unsigned)a, (unsigned)(a >> 32) & 0xFFFFu, WS_USED, 0x00020000u};
}
#define ALD16(dst, voff, desc, soff) asm volatile("buffer_load_dwordx4 %0, %1, %2, %3 offen sc1" : "=&v"(dst) : "v"(voff), "s"(desc), "s"(soff) : "memory")
#define ALD8(dst, voff, desc, soff) asm volatile("buffer_load_dwordx2 %0, %1, %2, %3 offen sc1" : "=&v"(dst) : "v"(voff), "s"(desc), "s"(soff) : "memory")
#define ALD4(dst, voff, desc, soff) asm volatile("buffer_load_dword %0, %1, %2, %3 offen sc1" : "=&v"(dst) : "v"(voff), "s"(desc), "s"(soff) : "memory")
#define KEEP(x) asm volatile("" ::"v"(x))
#define PINW(w) _Pragma("unroll") for (int k_ = 0; k_ < 7; ++k_) asm volatile("" : "+v"((w)[k_]))
#define BAR() do { asm volatile("s_waitcnt lgkmcnt(0)" ::: "memory"); __builtin_amdgcn_s_barrier(); asm volatile("" ::: "memory"); } while (0)
#define VMWAIT0() asm volatile("s_waitcnt vmcnt(0)" ::: "memory")
#define SCHED() __builtin_amdgcn_sched_barrier(0)

__device__ __forceinline__ bool any_sent4(u32x4 v) {
  unsigned m = max(max(v.x, v.y), max(v.z, v.w));
  return __builtin_amdgcn_ballot_w64(m == SENT) != 0ull;
}
__device__ __forceinline__ bool any_sent2(u32x2 a, u32x2 b) {
  unsigned m = max(max(a.x, a.y), max(b.x, b.y));
  return __builtin_amdgcn_ballot_w64(m == SENT) != 0ull;
}
__device__ __forceinline__ bool any_sent8(u32x4 a, u32x4 b) {
  unsigned m = max(max(max(a.x, a.y), max(a.z, a.w)), max(max(b.x, b.y), max(b.z, b.w)));
  return __builtin_amdgcn_ballot_w64(m == SENT) != 0ull;
}
__device__ __forceinline__ unsigned mbase(int s, int pol) {
  return OFF_MB + (unsigned)s * 131072u + (((unsigned)pol >> (s >> 4)) & 1u) * 8192u;
}
__device__ __forceinline__ int wait_flags2(rsrc_t rs, unsigned off0, unsigned off1, int need, int& tmo) {
  const int lane = threadIdx.x & 63;
  const unsigned vo = (lane & 1) ? off1 : off0;
  int lo = 0;
  for (unsigned sp = 0;; ++sp) {
    asm volatile("" ::: "memory");
    int v = (int)ld4(rs, vo, 0);
    lo = min(__builtin_amdgcn_readlane(v, 0), __builtin_amdgcn_readlane(v, 1));
    if (lo >= need) break;
    if (sp > SPIN_MAX || tmo) { tmo = 1; lo = NSEG + 1; break; }
    __builtin_amdgcn_s_sleep(4);
  }
  return lo;
}

__global__ void __launch_bounds__(512, 2) prep_kernel(Params P) {
  extern __shared__ __attribute__((aligned(16))) unsigned char smem[];
  LAS unsigned char* lds = (LAS unsigned char*)smem;
  const int tid = threadIdx.x, lane = tid & 63, wave = __builtin_amdgcn_readfirstlane(tid >> 6);
  const int b = blockIdx.x, xcd = b & 7, j = b >> 3;
  const int wh = xcd * 4 + (j >> 3), which = wh >> 4, h = wh & 15, rb = (j >> 2) & 1, cq = j & 3;
  const int n = lane & 15, g = lane >> 4;
  const rsrc_t rs = ws_rsrc(P.ws);
  if (which == 0) {
#pragma unroll
    for (int k = 0; k < 7; ++k) {
      const int fr = wave * 7 + k;
      const int ct = 7 * cq + (fr >> 3), ke = fr & 7;
      const int dp = 32 * (ct >> 1) + 8 * (n >> 2) + 4 * (ct & 1) + (n & 3);
      glds16(P.Wq + ((size_t)(dp * 16 + h) * 256 + 32 * ke + 8 * g), lds + fr * 1024);
    }
  } else {
#pragma unroll
    for (int k = 0; k < 7; ++k) {
      const int i = (wave * 7 + k) * 64 + lane;
      const int e = i / 14, ch = i - e * 14;
      glds16(P.Wo + ((size_t)(h * 256 + e) * 448 + 112 * cq + 8 * ch), lds + (wave * 7 + k) * 1024);
    }
  }
  if (wave == 7) {
    u32x4 xv[8];
    unsigned short wv[7][8];
    unsigned mv[4];
    if (b >= 8) {
#pragma unroll
      for (int k = 0; k < 8; ++k) {
        const int fr = min(b - 8 + 248 * k, 64 * 28 - 1), s = fr / 28, kq = fr - s * 28, q = kq >= 14 ? 1 : 0, k2 = kq - 14 * q;
        xv[k] = *(const u32x4*)(P.x + (size_t)(32 * s + 16 * q + n) * DM + 32 * k2 + 8 * g);
      }
#pragma unroll
      for (int k = 0; k < 7; ++k) {
        const int fr = min(b - 8 + 248 * k, 28 * 56 - 1), cb = fr / 56, u = fr - cb * 56;
#pragma unroll
        for (int jj = 0; jj < 8; ++jj) wv[k][jj] = P.Wd[(size_t)(32 * u + 16 * (jj >> 2) + 4 * g + (jj & 3)) * DM + 16 * cb + n];
      }
    } else if (b < 7) {
#pragma unroll
      for (int k = 0; k < 4; ++k) {
        const int i = k * 64 + lane, gg = (i >> 6) & 3, hi = (i >> 5) & 1, r = (i >> 2) & 7, p = i & 3;
        mv[k] = *(const unsigned*)(P.mem0 + r * DM + 64 * b + 16 * gg + 4 * p + 2 * hi);
      }
    }
    VMWAIT0();
    __syncthreads();
    if (b >= 8) {
#pragma unroll
      for (int k = 0; k < 8; ++k) {
        const int fr = b - 8 + 248 * k;
        if (fr < 64 * 28) st16(xv[k], rs, (unsigned)lane * 16u, OFF_XF + (unsigned)fr * 1024u);
      }
#pragma unroll
      for (int k = 0; k < 7; ++k) {
        const int fr = b - 8 + 248 * k;
        const u32x4 o = {wv[k][0] | ((unsigned)wv[k][1] << 16), wv[k][2] | ((unsigned)wv[k][3] << 16), wv[k][4] | ((unsigned)wv[k][5] << 16), wv[k][6] | ((unsigned)wv[k][7] << 16)};
        if (fr < 28 * 56) st16(o, rs, (unsigned)lane * 16u, OFF_WD + (unsigned)fr * 1024u);
      }
    } else if (b < 7) {
#pragma unroll
      for (int k = 0; k < 4; ++k)
#pragma unroll
        for (int c = 0; c < 4; ++c) st4(san(mv[k]), rs, (unsigned)(k * 64 + lane) * 4u, OFF_MB + K_M + (unsigned)c * 8192u + (unsigned)b * 1024u);
    }
  }
  bf16x8 a[2][8];
  const int kk = 7 * rb + (wave < 7 ? wave : 0);
  if (wave < 7) {
    const unsigned short* W1 = which ? P.Wv : P.Wk;
#pragma unroll
    for (int T = 0; T < 2; ++T) {
      const int d = 32 * kk + 8 * (n >> 2) + 4 * T + (n & 3);
      const unsigned short* p = W1 + ((size_t)(d * 16 + h) * 256 + 8 * g);
#pragma unroll
      for (int ke = 0; ke < 8; ++ke) a[T][ke] = *(const bf16x8*)(p + 32 * ke);
    }
    VMWAIT0();
    __syncthreads();
  }
  {
    const u32x4 sv = {SENT, SENT, SENT, SENT};
#pragma unroll
    for (int k = 0; k < 4; ++k) {
      const unsigned o = (unsigned)b * 32768u + (unsigned)(k * 512 + tid) * 16u;
      if (!(o >= K_M && o < K_M + 32768u)) st16(sv, rs, OFF_MB + o, 0);
    }
    if (b == 0 && tid == 0) *(unsigned*)(P.ws + FL_ROT) = (unsigned)__builtin_amdgcn_s_getreg(20 | (3 << 11)) & 7u;
    if (b == 3 && tid < (int)FL_COUNT) st4(tid == (int)FL_COUNT - 1 ? SENT : 0u, rs, OFF_FL + (unsigned)tid * FS, 0);
  }
  if (wave == 7) return;
  f32x4 acc[2][7];
#pragma unroll
  for (int c = 0; c < 7; ++c) { acc[0][c] = f32x4{0, 0, 0, 0}; acc[1][c] = f32x4{0, 0, 0, 0}; }
  if (which == 0) {
#pragma unroll
    for (int ke = 0; ke < 8; ++ke)
#pragma unroll
      for (int c = 0; c < 7; ++c) {
        const bf16x8 bb = *(const LAS bf16x8*)(lds + (c * 8 + ke) * 1024 + lane * 16);
        acc[0][c] = mfma32(a[0][ke], bb, acc[0][c]);
        acc[1][c] = mfma32(a[1][ke], bb, acc[1][c]);
      }
  } else {
    const LAS unsigned char* tb = lds + (8 * g + (n >> 2)) * 224 + (n & 3) * 8;
#pragma unroll
    for (int ke = 0; ke < 8; ++ke)
#pragma unroll
      for (int c = 0; c < 7; ++c) {
        typedef short v4s __attribute__((ext_vector_type(4)));
        const v4s lo = __builtin_amdgcn_ds_read_tr16_b64_v4i16((LAS v4s*)(tb + ke * 32 * 224 + c * 32));
        const v4s hi = __builtin_amdgcn_ds_read_tr16_b64_v4i16((LAS v4s*)(tb + ke * 32 * 224 + 4 * 224 + c * 32));
        const bf16x8 bb = __builtin_bit_cast(bf16x8, __builtin_shufflevector(lo, hi, 0, 1, 2, 3, 4, 5, 6, 7));
        acc[0][c] = mfma32(a[0][ke], bb, acc[0][c]);
        acc[1][c] = mfma32(a[1][ke], bb, acc[1][c]);
      }
  }
  const float sc = which ? 1.0f : 0.0625f * 1.4426950408889634f;
  const unsigned dst = (which ? OFF_WV : OFF_WK) + (unsigned)((h * 28 + 7 * cq) * 14 + kk) * 1024u;
#pragma unroll
  for (int c = 0; c < 7; ++c) {
    const f32x4 u = acc[0][c] * sc, v = acc[1][c] * sc;
    const u32x4 o = {pk(u[0], u[1]), pk(u[2], u[3]), pk(v[0], v[1]), pk(v[2], v[3])};
    st16(o, rs, (unsigned)lane * 16u, dst + (unsigned)c * 14u * 1024u);
  }
}

__device__ __forceinline__ u32x4 gather_m(u32x4 dsc, unsigned voff, unsigned so0, LAS unsigned char* dst, bool act, int& tmo) {
  u32x4 va, vb;
  ALD16(va, voff, dsc, so0);
  const unsigned so1 = so0 + K_REP;
  delay_c<D_GAP>();
  ALD16(vb, voff, dsc, so1);
  asm volatile("s_waitcnt vmcnt(1)" : "+v"(va)::"memory");
  if (__builtin_expect(any_sent4(va), 0)) {
    asm volatile("s_waitcnt vmcnt(0)" : "+v"(vb)::"memory");
    va = vb;
    for (unsigned sp = 2; any_sent4(va); ++sp) {
      asm volatile("" ::: "memory");
      const unsigned r = sp % 3u, so = r == 2u ? (so0 ^ 8192u) : so0 + r * K_REP;
      ALD16(va, voff, dsc, so);
      asm volatile("s_waitcnt vmcnt(0)" : "+v"(va)::"memory");
      if (sp > SPIN_MAX || tmo) { tmo = 1; break; }
    }
  }
  if (act) *(LAS u32x4*)dst = va;
  return vb;
}

__device__ __forceinline__ void probe_pol(const Params& P) {
  if (threadIdx.x >= 64) return;
  const rsrc_t rs = ws_rsrc(P.ws);
  __builtin_amdgcn_s_sleep(64);
  unsigned pol = 0;
  for (int round = 0; round < 3; ++round) {
    int dsum = 0;
#pragma unroll
    for (int p = 0; p < 4; ++p) {
      unsigned t[2];
#pragma unroll
      for (int cls = 0; cls < 2; ++cls) {
        const unsigned off = OFF_MB + (unsigned)(p * 16 + round) * 131072u + K_PROBE + (unsigned)cls * 8192u;
        const unsigned long long c0 = __builtin_amdgcn_s_memtime();
        unsigned v = ld4(rs, 0, off);
        asm volatile("s_waitcnt vmcnt(0)" : "+v"(v)::"memory");
        const unsigned long long c1 = __builtin_amdgcn_s_memtime();
        t[cls] = (unsigned)(c1 - c0);
      }
      const int d = max(-300, min(300, (int)t[0] - (int)t[1]));
      dsum += (p & 1) ? -d : d;
    }
    pol = 0;
#pragma unroll
    for (int p = 0; p < 4; ++p) pol |= (((dsum > 0) ? 1u : 0u) ^ (unsigned)(p & 1)) << p;
    if (dsum > 450 || dsum < -450) break;
  }
  if (threadIdx.x == 0) {
    st4(pol, rs, FL_POL, 0);
  }
}
__device__ __forceinline__ int get_pol(rsrc_t rs, int& tmo) {
  unsigned v;
  for (unsigned sp = 0;; ++sp) {
    asm volatile("" ::: "memory");
    v = (unsigned)__builtin_amdgcn_readfirstlane((int)ld4(rs, 0, FL_POL));
    if (v != SENT) break;
    if (sp > SPIN_MAX) { tmo = 1; v = 0; break; }
    __builtin_amdgcn_s_sleep(4);
  }
  return (int)v;
}

__device__ __forceinline__ void score_main(const Params& P, int idx, int wave, LAS unsigned char* lds) {
  const int lane = threadIdx.x & 63;
  const int h = idx >> 1, q = idx & 1;
  const rsrc_t rs = ws_rsrc(P.ws);
  LAS unsigned char* img = lds;
  LAS float* red = (LAS float*)(lds + 8192);
  const int g = lane >> 4, n = lane & 15;
  const int idxv = (n & 8) ? 0xEEEE : 0x4444;
  s16x4 E;
#pragma unroll
  for (int jj = 0; jj < 4; ++jj) E[jj] = (((4 * g + jj) & 7) == n) ? (short)0x3F80 : (short)0;
  int tmo = 0;
  bf16x16 w0[7], w1[7];
  const int Pp = 7 * q + wave;
  {
    const unsigned char* b0 = P.ws + OFF_WK + ((size_t)(h * 28 + 2 * Pp) * 14) * 1024 + lane * 16;
#pragma unroll
    for (int ks = 0; ks < 7; ++ks) {
      w0[ks] = cat16(*(const bf16x8*)(b0 + (2 * ks) * 1024), *(const bf16x8*)(b0 + (2 * ks + 1) * 1024));
      w1[ks] = cat16(*(const bf16x8*)(b0 + (14 + 2 * ks) * 1024), *(const bf16x8*)(b0 + (15 + 2 * ks) * 1024));
    }
  }
  const int pol = get_pol(rs, tmo);
  const unsigned gloc = (unsigned)(wave * 56 + (lane < 56 ? lane : 55)) * 16u;
  const unsigned gvoff = gloc + K_M;
  LAS unsigned char* gdst = img + gloc;
  const LAS unsigned char* afp = img + lane * 16;
  const LAS unsigned char* mp = img + (Pp >> 1) * 1024 + (2 * (Pp & 1) + (g >> 1)) * 256 + (n & 7) * 16 + (g & 1) * 8;
  const unsigned mpa = (unsigned)(size_t)mp;
  LAS float* redw = red + wave * 64 + n * 8 + 4 * g;
  const unsigned xfv = OFF_XF + (unsigned)Pp * 1024u + lane * 16, xfv1 = xfv + 14 * 1024;
  LAS float* red2 = (LAS float*)(lds + 12288);
  LAS float* red2w = red2 + wave * 256 + (g & 1) * 64 + n * 4;
  const LAS float* red2r = red2 + lane * 4;
  const unsigned sxmv = OFF_KM + (unsigned)idx * 1024u + lane * 16;
  const unsigned smv = K_SM + (unsigned)idx * 256u + lane * 4u;
  PINW(w0); PINW(w1);
  const u32x4 dsc = ws_desc(P.ws);
  u32x4 xf0, xf1;
  { const unsigned so = 0; ALD16(xf0, xfv, dsc, so); ALD16(xf1, xfv1, dsc, so); }
  for (int s = 0; s < NSEG; ++s) {
    const unsigned mb = mbase(s, pol);
    if (s > 0) delay_c<D_MS>();
    const u32x4 pend = gather_m(dsc, gvoff, mb, gdst, lane < 56, tmo);
    BAR();
    f32x4 c0, c1;
    u32x4 mf;
    {
      u32x2 mlo, mhi;
      asm volatile("ds_read2_b32 %0, %2 offset0:0 offset1:32\n\tds_read2_b32 %1, %2 offset0:1 offset1:33" : "=&v"(mlo), "=&v"(mhi) : "v"(mpa) : "memory");
      bf16x8 a[7];
#pragma unroll
      for (int ks = 0; ks < 7; ++ks) a[ks] = *(const LAS bf16x8*)(afp + ks * 1024);
      SCHED();
      c0 = f32x4{0, 0, 0, 0}; c1 = f32x4{0, 0, 0, 0};
#pragma unroll
      for (int ks = 0; ks < 7; ++ks) {
        c0 = smf(a[ks], w0[ks], c0, idxv);
        c1 = smf(a[ks], w1[ks], c1, idxv);
      }
      SCHED();
      asm volatile("s_waitcnt lgkmcnt(0)" : "+v"(mlo), "+v"(mhi)::"memory");
      mf = u32x4{mlo.x, mlo.y, mhi.x, mhi.y};
    }
    SCHED();
    const u32x2 t0 = {pk(c0[0], c0[1]), pk(c0[2], c0[3])}, t1 = {pk(c1[0], c1[1]), pk(c1[2], c1[3])};
    const f32x4 z = {0, 0, 0, 0};
    SCHED();
    const f32x4 y0 = mfma16(as_s4(t0), E, z), y1 = mfma16(as_s4(t1), E, z);
    SCHED();
    const u32x4 kf = {pk(y0[0], y0[1]), pk(y0[2], y0[3]), pk(y1[0], y1[1]), pk(y1[2], y1[3])};
    const f32x4 S = mfma32(as_b8(kf), as_b8(mf), z);
    if (n < 8 && g < 2) *(LAS f32x4*)redw = S;
    VMWAIT0();
    KEEP(pend);
    BAR();
    if (wave == 0 && s < NSEG - 1) {
      const float v0 = red[lane], v1 = red[64 + lane], v2 = red[128 + lane], v3 = red[192 + lane], v4 = red[256 + lane], v5 = red[320 + lane], v6 = red[384 + lane];
      const float v = ((v0 + v1) + (v2 + v3)) + ((v4 + v5) + v6);
      const unsigned o = san(__builtin_bit_cast(unsigned, v));
      st4(o, rs, smv, mb);
      st4(o, rs, smv + K_REP, mb);
      st4(o, rs, smv, mb ^ 8192u);
    }
    asm volatile("" : "+v"(xf0), "+v"(xf1));
    {
      const f32x4 d0 = mfma32(as_b8(kf), as_b8(xf0), z), d1 = mfma32(as_b8(kf), as_b8(xf1), z);
      if (g < 2) { *(LAS f32x4*)red2w = d0; *(LAS f32x4*)(red2w + 128) = d1; }
    }
    BAR();
    if (wave == 1) {
      f32x4 v = *(const LAS f32x4*)red2r;
#pragma unroll
      for (int w = 1; w < 7; ++w) v += *(const LAS f32x4*)(red2r + w * 256);
      st16(__builtin_bit_cast(u32x4, v), rs, sxmv, (unsigned)s * KM_S);
    }
    if (s < NSEG - 1) delay_c<D_BULK>();
    { const unsigned so = (unsigned)min(s + 1, NSEG - 1) * XF_S; ALD16(xf0, xfv, dsc, so); ALD16(xf1, xfv1, dsc, so); }
  }
  VMWAIT0();
  BAR();
}

__device__ __forceinline__ void score_sx(const Params& P, int idx, LAS unsigned char* lds) {
  const int lane = threadIdx.x & 63;
  const int wave = 7;
  const int h = idx >> 1, q = idx & 1;
  const rsrc_t rs = ws_rsrc(P.ws);
  LAS unsigned char* img = lds;
  const int g = lane >> 4, n = lane & 15;
  const int idxv = (n & 8) ? 0xEEEE : 0x4444;
  s16x4 E;
#pragma unroll
  for (int jj = 0; jj < 4; ++jj) E[jj] = (((4 * g + jj) & 7) == n) ? (short)0x3F80 : (short)0;
  int tmo = 0;
  const int pol = get_pol(rs, tmo);
  const unsigned f0 = FL_T1K + (unsigned)(h * 2) * FS, f1 = f0 + FS;
  int known = wait_flags2(rs, f0, f1, 1, tmo);
  const unsigned flv = (lane & 1) ? f1 : f0;
  u32x4 kx[14];
  const unsigned kxv = OFF_KX + (unsigned)((h * 2 + q) * 14) * 1024u + lane * 16;
  const u32x4 dsc = ws_desc(P.ws);
  const unsigned kxa[4] = {kxv, kxv + 4096, kxv + 8192, kxv + 12288};
#define KXLD(k, so) asm volatile("buffer_load_dwordx4 %0, %1, %2, %3 offen offset:%c4 sc1" : "=&v"(kx[k]) : "v"(kxa[(k) >> 2]), "s"(dsc), "s"(so), "i"(((k) & 3) * 1024) : "memory")
  {
    const unsigned so = 0;
    KXLD(0, so); KXLD(1, so); KXLD(2, so); KXLD(3, so); KXLD(4, so); KXLD(5, so); KXLD(6, so);
    KXLD(7, so); KXLD(8, so); KXLD(9, so); KXLD(10, so); KXLD(11, so); KXLD(12, so); KXLD(13, so);
  }
  const unsigned gloc = (unsigned)(wave * 56 + (lane < 56 ? lane : 55)) * 16u;
  const unsigned gvoff = gloc + K_M;
  LAS unsigned char* gdst = img + gloc;
  const LAS unsigned char* afp = img + lane * 16;
  const unsigned sxv = K_SX + (unsigned)idx * 256u + (unsigned)(g * 8 + (n & 7)) * 8u;
  const unsigned chf = FL_CH + (unsigned)idx * FS;
  __builtin_amdgcn_s_setprio(3);
  for (int s = 0; s < NSEG; ++s) {
    const unsigned mb = mbase(s, pol);
    if (s > 0) delay_c<D_MS>();
    const u32x4 pend = gather_m(dsc, gvoff, mb, gdst, lane < 56, tmo);
    int fl;
    { const unsigned z0 = 0; ALD4(fl, flv, dsc, z0); }
    BAR();
    {
      bf16x8 a[7];
#pragma unroll
      for (int ks = 0; ks < 7; ++ks) a[ks] = *(const LAS bf16x8*)(afp + ks * 1024);
      SCHED();
      f32x4 c0 = {0, 0, 0, 0};
#pragma unroll
      for (int ks = 0; ks < 7; ++ks) c0 = smf(a[ks], cat16(as_b8(kx[2 * ks]), as_b8(kx[2 * ks + 1])), c0, idxv);
      asm volatile("s_waitcnt vmcnt(0)" : "+v"(fl)::"memory");
      KEEP(pend);
      const u32x2 t0 = {pk(c0[0], c0[1]), pk(c0[2], c0[3])};
      const f32x4 z = {0, 0, 0, 0};
      const f32x4 y0 = mfma16(as_s4(t0), E, z);
      const u32x2 o = {san(pk(y0[0], y0[1])), san(pk(y0[2], y0[3]))};
      if (n < 8 && s < NSEG - 1) {
        st8(o, rs, sxv, mb);
        st8(o, rs, sxv + K_REP, mb);
        st8(o, rs, sxv, mb ^ 8192u);
      }
      if (lane == 0) st4((unsigned)s, rs, chf, 0);
      SCHED();
    }
    BAR();
    BAR();
    if (s < NSEG - 1) delay_c<D_BULK>();
    const int sn = min(s + 1, NSEG - 2);
    known = max(known, min(__builtin_amdgcn_readlane(fl, 0), __builtin_amdgcn_readlane(fl, 1)));
    if (__builtin_expect(known <= sn, 0)) { known = wait_flags2(rs, f0, f1, sn + 1, tmo); }
    {
      const unsigned so = (unsigned)sn * KX_S;
      KXLD(0, so); KXLD(1, so); KXLD(2, so); KXLD(3, so); KXLD(4, so); KXLD(5, so); KXLD(6, so);
      KXLD(7, so); KXLD(8, so); KXLD(9, so); KXLD(10, so); KXLD(11, so); KXLD(12, so); KXLD(13, so);
    }
    SCHED();
  }
  VMWAIT0();
  BAR();
  if (lane == 0) st4((unsigned)NSEG, rs, chf, 0);
}

__device__ __forceinline__ void value_wg(const Params& P, int cb, LAS unsigned char* lds) {
  const int tid = threadIdx.x, lane = tid & 63, wave = __builtin_amdgcn_readfirstlane(tid >> 6);
  const rsrc_t rs = ws_rsrc(P.ws);
  LAS unsigned char* img = lds;
  LAS float* red = (LAS float*)(lds + 8192);
  const int g = lane >> 4, n = lane & 15, sel = n >> 3, i = n & 7;
  const int idxv = (n & 8) ? 0xEEEE : 0x4444;
  const int h0 = 2 * wave, hs = h0 + sel;
  int tmo = 0;
  bf16x16 w0[7], w1[7];
  {
    const unsigned char* b0 = P.ws + OFF_WV + ((size_t)(h0 * 28 + cb) * 14) * 1024 + lane * 16;
    const unsigned char* b1 = b0 + (size_t)28 * 14 * 1024;
#pragma unroll
    for (int ks = 0; ks < 7; ++ks) {
      w0[ks] = cat16(*(const bf16x8*)(b0 + (2 * ks) * 1024), *(const bf16x8*)(b0 + (2 * ks + 1) * 1024));
      w1[ks] = cat16(*(const bf16x8*)(b1 + (2 * ks) * 1024), *(const bf16x8*)(b1 + (2 * ks + 1) * 1024));
    }
  }
  const int pol = get_pol(rs, tmo);
  const unsigned fa = FL_T1V + (unsigned)(h0 * 2 + (cb >= 14 ? 1 : 0)) * FS, fb = fa + 2 * FS;
  int known = 0;
  const unsigned vxv = OFF_VX + (unsigned)(h0 * 28 + cb) * 1024u + lane * 16, vxv1 = vxv + 28 * 1024;
  const unsigned flv = (lane & 1) ? fb : fa;
  const unsigned gloc = (unsigned)(wave * 56 + (lane < 56 ? lane : 55)) * 16u;
  const unsigned gvoff = gloc + K_M;
  LAS unsigned char* gdst = img + gloc;
  const LAS unsigned char* afp = img + lane * 16;
  const unsigned vmv = OFF_VM + (unsigned)(h0 * 28 + cb) * 512u + lane * 8;
  const unsigned sxv = K_SX + (unsigned)(hs * 2) * 256u + (unsigned)(g * 8 + i) * 8u;
  const unsigned smv = K_SM + (unsigned)(hs * 2) * 256u + (unsigned)(i * 8 + 4 * (g & 1)) * 4u;
  LAS float* redw = red + wave * 128 + i * 16 + 4 * g;
  const LAS float* redr = red + ((lane >> 2) & 7) * 16 + 4 * (lane & 3) + 2 * (lane >> 5);
  const unsigned mv = K_M + (unsigned)cb * 256u + lane * 4u;
  const unsigned chf = FL_CH + (unsigned)(32 + cb) * FS;
  u32x2 vm0 = {0, 0}, vm1 = {0, 0};
  PINW(w0); PINW(w1);
  const u32x4 dsc = ws_desc(P.ws);
  for (int s = 0; s < NSEG - 1; ++s) {
    const unsigned mb = mbase(s, pol), mbn = mbase(s + 1, pol);
    int fl;
    { const unsigned z = 0; ALD4(fl, flv, dsc, z); }
    if (s > 0) { if (wave == 0) delay_c<D_MV0>(); else delay_c<D_MV>(); }
    const u32x4 pend = gather_m(dsc, gvoff, mb, gdst, lane < 56, tmo);
    asm volatile("" : "+v"(fl));
    BAR();
    known = max(known, min(__builtin_amdgcn_readlane(fl, 0), __builtin_amdgcn_readlane(fl, 1)));
    if (__builtin_expect(known <= s, 0)) { known = wait_flags2(rs, fa, fb, s + 1, tmo); }
    bf16x8 a[7];
#pragma unroll
    for (int ks = 0; ks < 7; ++ks) a[ks] = *(const LAS bf16x8*)(afp + ks * 1024);
    u32x4 vx0, vx1;
    {
      const unsigned so = (unsigned)s * VX_S;
      ALD16(vx0, vxv, dsc, so);
      ALD16(vx1, vxv1, dsc, so);
    }
    st8(vm0, rs, vmv, (unsigned)max(s - 1, 0) * VM_S);
    st8(vm1, rs, vmv + 28 * 512, (unsigned)max(s - 1, 0) * VM_S);
    if (wave == 1 && lane == 0) st4((unsigned)max(s - 1, 0), rs, chf, 0);
    SCHED();
    delay_c<D_SX>();
    u32x2 x0, x1, y0, y1;
    u32x4 ua, ub, wa, wb;
    const unsigned sxv1 = sxv + 256, smv1 = smv + 256, mb1 = mb + K_REP;
    ALD8(x0, sxv, dsc, mb);
    ALD8(x1, sxv1, dsc, mb);
    SCHED();
    f32x4 c0 = {0, 0, 0, 0}, c1 = {0, 0, 0, 0};
#pragma unroll
    for (int ks = 0; ks < 7; ++ks) {
      c0 = smf(a[ks], w0[ks], c0, idxv);
      c1 = smf(a[ks], w1[ks], c1, idxv);
    }
    vm0 = u32x2{pk(c0[0], c0[1]), pk(c0[2], c0[3])};
    vm1 = u32x2{pk(c1[0], c1[1]), pk(c1[2], c1[3])};
    SCHED();
    delay_c<D_G2>();
    ALD8(y0, sxv, dsc, mb1);
    ALD8(y1, sxv1, dsc, mb1);
    delay_c<D_SM>();
    ALD16(ua, smv, dsc, mb);
    ALD16(ub, smv1, dsc, mb);
    asm volatile("s_waitcnt vmcnt(4)" : "+v"(x0), "+v"(x1), "+v"(vx0), "+v"(vx1)::"memory");
    KEEP(pend);
    if (__builtin_expect(any_sent2(x0, x1), 0)) {
      asm volatile("s_waitcnt vmcnt(2)" : "+v"(y0), "+v"(y1)::"memory");
      x0 = y0; x1 = y1;
      for (unsigned sp = 2; any_sent2(x0, x1); ++sp) {
        asm volatile("" ::: "memory");
        const unsigned r = sp % 3u, so = r == 2u ? (mb ^ 8192u) : mb + r * K_REP;
        ALD8(x0, sxv, dsc, so);
        ALD8(x1, sxv1, dsc, so);
        asm volatile("s_waitcnt vmcnt(0)" : "+v"(x0), "+v"(x1)::"memory");
        if (sp > SPIN_MAX || tmo) { tmo = 1; break; }
      }
    }
    SCHED();
    float t[8] = {bflo(x0.x), bfhi(x0.x), bflo(x0.y), bfhi(x0.y), bflo(x1.x), bfhi(x1.x), bflo(x1.y), bfhi(x1.y)};
    float m1 = vmax(vmax(vmax(t[0], t[1]), vmax(t[2], t[3])), vmax(vmax(t[4], t[5]), vmax(t[6], t[7])));
    m1 = max32(max16(m1));
    float l1 = 0.f;
#pragma unroll
    for (int k = 0; k < 8; ++k) { t[k] = __builtin_amdgcn_exp2f(t[k] - m1); l1 += t[k]; }
    SCHED();
    ALD16(wa, smv, dsc, mb1);
    ALD16(wb, smv1, dsc, mb1);
    SCHED();
    l1 = sum32(sum16(l1));
    const u32x4 pb = {pk(t[0], t[1]), pk(t[2], t[3]), pk(t[4], t[5]), pk(t[6], t[7])};
    const f32x4 z = {0, 0, 0, 0};
    const f32x4 ax0 = mfma32(as_b8(vx0), as_b8(pb), z), ax1 = mfma32(as_b8(vx1), as_b8(pb), z);
    const f32x4 ax = sel ? ax1 : ax0;
    SCHED();
    asm volatile("s_waitcnt vmcnt(2)" : "+v"(ua), "+v"(ub)::"memory");
    KEEP(y0); KEEP(y1);
    if (__builtin_expect(any_sent8(ua, ub), 0)) {
      asm volatile("s_waitcnt vmcnt(0)" : "+v"(wa), "+v"(wb)::"memory");
      ua = wa; ub = wb;
      for (unsigned sp = 2; any_sent8(ua, ub); ++sp) {
        asm volatile("" ::: "memory");
        const unsigned r = sp % 3u, so = r == 2u ? (mb ^ 8192u) : mb + r * K_REP;
        ALD16(ua, smv, dsc, so);
        ALD16(ub, smv1, dsc, so);
        asm volatile("s_waitcnt vmcnt(0)" : "+v"(ua), "+v"(ub)::"memory");
        if (sp > SPIN_MAX || tmo) { tmo = 1; break; }
      }
    }
    const f32x4 sm = __builtin_bit_cast(f32x4, ua) + __builtin_bit_cast(f32x4, ub);
    float m2 = vmax(vmax(sm[0], sm[1]), vmax(sm[2], sm[3]));
    m2 = max16(m2);
    const float M = vmax(m1, m2);
    const float al = __builtin_amdgcn_exp2f(m1 - M);
    const float e0 = __builtin_amdgcn_exp2f(sm[0] - M), e1 = __builtin_amdgcn_exp2f(sm[1] - M), e2 = __builtin_amdgcn_exp2f(sm[2] - M), e3 = __builtin_amdgcn_exp2f(sm[3] - M);
    const u32x2 pm = {pk(e0, e1), pk(e2, e3)};
    const f32x4 cx = ax * al;
    const f32x4 r0 = mfma16(as_s4(vm0), as_s4(pm), cx), r1 = mfma16(as_s4(vm1), as_s4(pm), cx);
    const float l2 = sum16((e0 + e1) + (e2 + e3));
    const float rl = __builtin_amdgcn_rcpf(al * l1 + l2);
    f32x4 r = (sel ? r1 : r0) * rl;
#pragma unroll
    for (int k = 0; k < 4; ++k) {
      const float rk = r[k];
      const int o = __builtin_amdgcn_update_dpp(0, __builtin_bit_cast(int, rk), 0x128, 0xf, 0xf, true);
      r[k] += __builtin_bit_cast(float, o);
    }
    if (sel == 0) *(LAS f32x4*)redw = r;
    BAR();
    if (wave == 0) {
      float a0[8], a1[8];
#pragma unroll
      for (int w = 0; w < 8; ++w) { a0[w] = redr[w * 128]; a1[w] = redr[w * 128 + 1]; }
      const float s0 = ((a0[0] + a0[1]) + (a0[2] + a0[3])) + ((a0[4] + a0[5]) + (a0[6] + a0[7]));
      const float s1 = ((a1[0] + a1[1]) + (a1[2] + a1[3])) + ((a1[4] + a1[5]) + (a1[6] + a1[7]));
      const unsigned o = san(pk(s0, s1));
      st4(o, rs, mv, mbn);
      st4(o, rs, mv + K_REP, mbn);
      st4(o, rs, mv, mbn ^ 8192u);
      asm volatile("s_waitcnt vmcnt(3)" ::: "memory");
    } else {
      VMWAIT0();
    }
    KEEP(wa); KEEP(wb);
  }
  {
    const int s = NSEG - 1;
    const unsigned mb = mbase(s, pol);
    if (wave == 0) delay_c<D_MV0>(); else delay_c<D_MV>();
    const u32x4 pend = gather_m(dsc, gvoff, mb, gdst, lane < 56, tmo);
    BAR();
    st8(vm0, rs, vmv, (unsigned)(s - 1) * VM_S);
    st8(vm1, rs, vmv + 28 * 512, (unsigned)(s - 1) * VM_S);
    f32x4 c0 = {0, 0, 0, 0}, c1 = {0, 0, 0, 0};
#pragma unroll
    for (int ks = 0; ks < 7; ++ks) {
      const bf16x8 a = *(const LAS bf16x8*)(afp + ks * 1024);
      c0 = smf(a, w0[ks], c0, idxv);
      c1 = smf(a, w1[ks], c1, idxv);
    }
    vm0 = u32x2{pk(c0[0], c0[1]), pk(c0[2], c0[3])};
    vm1 = u32x2{pk(c1[0], c1[1]), pk(c1[2], c1[3])};
    st8(vm0, rs, vmv, (unsigned)s * VM_S);
    st8(vm1, rs, vmv + 28 * 512, (unsigned)s * VM_S);
    VMWAIT0();
    KEEP(pend);
    BAR();
    if (wave == 1 && lane == 0) st4((unsigned)NSEG, rs, chf, 0);
  }
}

__device__ __forceinline__ void stage_x(const Params& P, int s, LAS unsigned char* buf, int lane, int first, int count) {
  const unsigned char* p = P.ws + OFF_XF + (size_t)s * XF_S + lane * 16;
  for (int k = first; k < first + count; ++k) glds16(p + k * 1024, buf + k * 1024);
}

template <int MODE>
__device__ __forceinline__ void stream_wg(const Params& P, int id, LAS unsigned char* lds) {
  const int tid = threadIdx.x, lane = tid & 63, wave = __builtin_amdgcn_readfirstlane(tid >> 6);
  const rsrc_t rs = ws_rsrc(P.ws);
  const int n = lane & 15, g = lane >> 4;
  bf16x8 w0[14], w1[14];
  if (MODE == 2) {
#pragma unroll 1
    for (int m = 0; m < 2; ++m) {
      const unsigned short* W = m ? P.Wu : P.Wg;
      for (int k = wave; k < 98; k += 8) {
        const int i = k * 64 + lane, d = i / 14, ch = i - d * 14;
        glds16(W + (size_t)d * 1792 + 112 * id + 8 * ch, lds + k * 1024);
      }
      VMWAIT0();
      __syncthreads();
      if (wave < 7) {
        const LAS unsigned char* tb = lds + (8 * g + (n >> 2)) * 224 + (n & 3) * 8 + wave * 32;
#pragma unroll
        for (int kk = 0; kk < 14; ++kk) {
          typedef short v4s __attribute__((ext_vector_type(4)));
          const v4s lo = __builtin_amdgcn_ds_read_tr16_b64_v4i16((LAS v4s*)(tb + kk * 32 * 224));
          const v4s hi = __builtin_amdgcn_ds_read_tr16_b64_v4i16((LAS v4s*)(tb + kk * 32 * 224 + 4 * 224));
          const bf16x8 f = __builtin_bit_cast(bf16x8, __builtin_shufflevector(lo, hi, 0, 1, 2, 3, 4, 5, 6, 7));
          if (m == 0) w0[kk] = f; else w1[kk] = f;
        }
      }
      __syncthreads();
    }
  } else if (wave < 7) {
    const int h = id >> 1, hq = id & 1;
    const unsigned char* b0 = P.ws + (MODE ? OFF_WV : OFF_WK) + ((size_t)(h * 28 + 14 * hq + 2 * wave) * 14) * 1024 + lane * 16;
#pragma unroll
    for (int kk = 0; kk < 14; ++kk) { w0[kk] = *(const bf16x8*)(b0 + kk * 1024); w1[kk] = *(const bf16x8*)(b0 + (14 + kk) * 1024); }
  }
  const unsigned fl = (MODE == 0 ? FL_T1K : MODE == 1 ? FL_T1V : FL_F1) + (unsigned)id * FS;
  if (wave == 7) {
    stage_x(P, 0, lds, lane, 0, 28);
    stage_x(P, 1, lds + 28672, lane, 0, 28);
    asm volatile("s_waitcnt vmcnt(28)" ::: "memory");
    int nb = 2;
    for (int s = 0; s < NSEG; ++s) {
      BAR();
      if (s + 2 < NSEG) { stage_x(P, s + 2, lds + nb * 28672, lane, 0, 28); asm volatile("s_waitcnt vmcnt(28)" ::: "memory"); }
      else VMWAIT0();
      nb = nb == 2 ? 0 : nb + 1;
    }
    BAR();
    if (lane == 0) st4((unsigned)NSEG, rs, fl, 0);
  } else {
    int cbuf = 0;
    for (int s = 0; s < NSEG; ++s) {
      BAR();
      if (wave == 0 && s > 0 && lane == 0) st4((unsigned)(s <= NOLAG ? s : s - 1), rs, fl, 0);
      const LAS unsigned char* xb = lds + cbuf * 28672 + lane * 16;
      cbuf = cbuf == 2 ? 0 : cbuf + 1;
      f32x4 c00 = {0, 0, 0, 0}, c01 = {0, 0, 0, 0}, c10 = {0, 0, 0, 0}, c11 = {0, 0, 0, 0};
      bf16x8 xa[3], xc[3];
      xa[0] = *(const LAS bf16x8*)(xb); xc[0] = *(const LAS bf16x8*)(xb + 14 * 1024);
      xa[1] = *(const LAS bf16x8*)(xb + 1024); xc[1] = *(const LAS bf16x8*)(xb + 15 * 1024);
#pragma unroll
      for (int kk = 0; kk < 14; ++kk) {
        if (kk + 2 < 14) { xa[(kk + 2) % 3] = *(const LAS bf16x8*)(xb + (kk + 2) * 1024); xc[(kk + 2) % 3] = *(const LAS bf16x8*)(xb + (16 + kk) * 1024); }
        SCHED();
        const bf16x8 x0 = xa[kk % 3], x1 = xc[kk % 3];
        if (MODE == 1) { c00 = mfma32(x0, w0[kk], c00); c01 = mfma32(x0, w1[kk], c01); c10 = mfma32(x1, w0[kk], c10); c11 = mfma32(x1, w1[kk], c11); }
        else { c00 = mfma32(w0[kk], x0, c00); c01 = mfma32(w1[kk], x0, c01); c10 = mfma32(w0[kk], x1, c10); c11 = mfma32(w1[kk], x1, c11); }
        SCHED();
      }
      if (MODE == 2) {
#pragma unroll
        for (int q = 0; q < 2; ++q) {
          const f32x4 cg = q ? c10 : c00, cu = q ? c11 : c01;
          float hv[4];
#pragma unroll
          for (int k = 0; k < 4; ++k) {
            const float gg = cg[k];
            hv[k] = gg * __builtin_amdgcn_rcpf(1.0f + __builtin_amdgcn_exp2f(-1.4426950408889634f * gg)) * cu[k];
          }
          const u32x2 o = {pk(hv[0], hv[1]), pk(hv[2], hv[3])};
          st8(o, rs, OFF_HF + (unsigned)(q * 112 + 7 * id + wave) * 512u + lane * 8, (unsigned)s * HF_S);
        }
      } else if (MODE == 0) {
        const int h = id >> 1, Pp = 7 * (id & 1) + wave;
        const u32x4 o0 = {pk(c00[0], c00[1]), pk(c00[2], c00[3]), pk(c01[0], c01[1]), pk(c01[2], c01[3])};
        const u32x4 o1 = {pk(c10[0], c10[1]), pk(c10[2], c10[3]), pk(c11[0], c11[1]), pk(c11[2], c11[3])};
        st16(o0, rs, OFF_KX + (unsigned)((h * 2 + 0) * 14 + Pp) * 1024u + lane * 16, (unsigned)s * KX_S);
        st16(o1, rs, OFF_KX + (unsigned)((h * 2 + 1) * 14 + Pp) * 1024u + lane * 16, (unsigned)s * KX_S);
      } else {
        const int h = id >> 1, cb = 14 * (id & 1) + 2 * wave;
        const u32x4 o0 = {pk(c00[0], c00[1]), pk(c00[2], c00[3]), pk(c10[0], c10[1]), pk(c10[2], c10[3])};
        const u32x4 o1 = {pk(c01[0], c01[1]), pk(c01[2], c01[3]), pk(c11[0], c11[1]), pk(c11[2], c11[3])};
        st16(o0, rs, OFF_VX + (unsigned)(h * 28 + cb) * 1024u + lane * 16, (unsigned)s * VX_S);
        st16(o1, rs, OFF_VX + (unsigned)(h * 28 + cb + 1) * 1024u + lane * 16, (unsigned)s * VX_S);
      }
      if (s < NOLAG) VMWAIT0(); else asm volatile("s_waitcnt vmcnt(2)" ::: "memory");
    }
    VMWAIT0();
    BAR();
  }
}

template <int SLEEP>
__device__ __forceinline__ void wait_lanes(rsrc_t rs, unsigned voff, int need, int& tmo) {
  for (unsigned sp = 0;; ++sp) {
    asm volatile("" ::: "memory");
    const int v = (int)ld4(rs, voff, 0);
    if (__builtin_amdgcn_ballot_w64(v < need) == 0ull) break;
    if (sp > SPIN_MAX || tmo) { tmo = 1; break; }
    __builtin_amdgcn_s_sleep(SLEEP);
  }
}

__device__ __forceinline__ void task_sxx(const Params& P, int s, LAS unsigned char* lds, int& tmo) {
  int tid = threadIdx.x;
  asm volatile("" : "+v"(tid));
  const int lane = tid & 63, wave = __builtin_amdgcn_readfirstlane(tid >> 6);
  const rsrc_t rs = ws_rsrc(P.ws);
  __syncthreads();
  stage_x(P, s, lds, lane, wave * 4, wave < 7 ? 4 : 0);
  if (wave == 0) wait_lanes<24>(rs, FL_T1K + (unsigned)(lane & 31) * FS, s + 1, tmo);
  BAR();
  const unsigned kv = lane * 16;
  const unsigned so = OFF_KX + (unsigned)(2 * wave * 2 * 14) * 1024u + (unsigned)s * KX_S;
  u32x4 ka[2][7], kb[2][7];
#pragma unroll
  for (int k = 0; k < 7; ++k) { ka[0][k] = ld16(rs, kv, so + k * 1024); kb[0][k] = ld16(rs, kv, so + (14 + k) * 1024); }
  asm volatile("s_waitcnt vmcnt(14)" ::: "memory");
  __syncthreads();
  const LAS unsigned char* xb = lds + lane * 16;
  f32x4 c00 = {0, 0, 0, 0}, c01 = {0, 0, 0, 0}, c11 = {0, 0, 0, 0};
#pragma unroll
  for (int c = 0; c < 4; ++c) {
    if (c + 1 < 4) {
      const int hn = (c + 1) >> 1, kn = ((c + 1) & 1) * 7;
#pragma unroll
      for (int k = 0; k < 7; ++k) { ka[(c + 1) & 1][k] = ld16(rs, kv, so + (hn * 28 + kn + k) * 1024); kb[(c + 1) & 1][k] = ld16(rs, kv, so + (hn * 28 + 14 + kn + k) * 1024); }
    }
    SCHED();
#pragma unroll
    for (int k = 0; k < 7; ++k) {
      const int kk = (c & 1) * 7 + k;
      const bf16x8 x0 = *(const LAS bf16x8*)(xb + kk * 1024), x1 = *(const LAS bf16x8*)(xb + (14 + kk) * 1024);
      c00 = mfma32(as_b8(ka[c & 1][k]), x0, c00);
      c01 = mfma32(as_b8(ka[c & 1][k]), x1, c01);
      c11 = mfma32(as_b8(kb[c & 1][k]), x1, c11);
    }
    if (c & 1) {
      const unsigned o = OFF_SXX + (unsigned)((2 * wave + (c >> 1)) * 3) * 1024u + (unsigned)s * SXX_S;
      st16(__builtin_bit_cast(u32x4, c00), rs, kv, o);
      st16(__builtin_bit_cast(u32x4, c01), rs, kv, o + 1024);
      st16(__builtin_bit_cast(u32x4, c11), rs, kv, o + 2048);
      c00 = f32x4{0, 0, 0, 0}; c01 = f32x4{0, 0, 0, 0}; c11 = f32x4{0, 0, 0, 0};
    }
    SCHED();
  }
  VMWAIT0();
  __syncthreads();
  if (tid == 0) st4(1u, rs, FL_T2 + (unsigned)s * FS, 0);
}

__device__ __forceinline__ void task_ffn2(const Params& P, int sp, LAS unsigned char* lds, int& tmo) {
  int tid = threadIdx.x;
  asm volatile("" : "+v"(tid));
  const int lane = tid & 63, wave = __builtin_amdgcn_readfirstlane(tid >> 6);
  const rsrc_t rs = ws_rsrc(P.ws);
  __syncthreads();
  const unsigned char* wd = P.ws + OFF_WD + (size_t)(4 * (wave < 7 ? wave : 0)) * 56 * 1024 + lane * 16;
  bf16x8 wf[4][4];
#pragma unroll
  for (int uu = 0; uu < 4; ++uu)
#pragma unroll
    for (int c = 0; c < 4; ++c) wf[uu][c] = *(const bf16x8*)(wd + (size_t)(c * 56 + uu) * 1024);
  if (wave == 0) wait_lanes<24>(rs, FL_F1 + (unsigned)(lane & 15) * FS, 2 * sp + 2, tmo);
  BAR();
  f32x4 acc[4][4];
#pragma unroll
  for (int c = 0; c < 4; ++c)
#pragma unroll
    for (int q = 0; q < 4; ++q) acc[c][q] = f32x4{0, 0, 0, 0};
#pragma unroll 1
  for (int half = 0; half < 2; ++half) {
    if (half) __syncthreads();
    {
      u32x4 v[14];
#pragma unroll
      for (int k = 0; k < 14; ++k) {
        const int pc = wave * 14 + k, tile = pc / 28, f2 = pc - tile * 28;
        v[k] = ld16(rs, lane * 16, OFF_HF + (unsigned)(2 * sp + (tile >> 1)) * HF_S + (unsigned)((tile & 1) * 112 + 56 * half + 2 * f2) * 512u);
      }
#pragma unroll
      for (int k = 0; k < 14; ++k) *(LAS u32x4*)(lds + (wave * 14 + k) * 1024 + lane * 16) = v[k];
    }
    __syncthreads();
    if (wave < 7) {
      const LAS unsigned char* hb = lds + lane * 8;
#pragma unroll 1
      for (int u0 = 0; u0 < 28; u0 += 4) {
        const int ug = 28 * half + u0;
        const int un = ug + 4 < 56 ? ug + 4 : ug;
#pragma unroll
        for (int uu = 0; uu < 4; ++uu) {
          const int u = u0 + uu;
          u32x4 hf[4];
#pragma unroll
          for (int q = 0; q < 4; ++q) {
            const u32x2 lo = *(const LAS u32x2*)(hb + (q * 56 + 2 * u) * 512), hi = *(const LAS u32x2*)(hb + (q * 56 + 2 * u + 1) * 512);
            hf[q] = u32x4{lo.x, lo.y, hi.x, hi.y};
          }
#pragma unroll
          for (int q = 0; q < 4; ++q)
#pragma unroll
            for (int c = 0; c < 4; ++c) acc[c][q] = mfma32(wf[uu][c], as_b8(hf[q]), acc[c][q]);
          SCHED();
#pragma unroll
          for (int c = 0; c < 4; ++c) wf[uu][c] = *(const bf16x8*)(wd + (size_t)(c * 56 + un + uu) * 1024);
          SCHED();
        }
      }
    } else if (half == 0) {
      const unsigned short* xp = P.x + (size_t)(64 * sp + lane) * DM;
      float acc1 = 0.f;
#pragma unroll 7
      for (int k = 0; k < 56; ++k) {
        const u32x4 xv = *(const u32x4*)(xp + 8 * k), gv = *(const u32x4*)(P.gw + 8 * k);
        acc1 += bflo(xv.x) * bflo(gv.x) + bfhi(xv.x) * bfhi(gv.x) + bflo(xv.y) * bflo(gv.y) + bfhi(xv.y) * bfhi(gv.y) +
                bflo(xv.z) * bflo(gv.z) + bfhi(xv.z) * bfhi(gv.z) + bflo(xv.w) * bflo(gv.w) + bfhi(xv.w) * bfhi(gv.w);
      }
      const float gate = __builtin_amdgcn_rcpf(1.0f + __builtin_amdgcn_exp2f(-1.4426950408889634f * acc1));
      st4(__builtin_bit_cast(unsigned, gate), rs, (unsigned)lane * 4u, OFF_G + (unsigned)(64 * sp) * 4u);
    }
  }
  if (wave < 7) {
#pragma unroll
    for (int c = 0; c < 4; ++c)
#pragma unroll
      for (int q = 0; q < 4; ++q) {
        const u32x2 o = {pk(acc[c][q][0], acc[c][q][1]), pk(acc[c][q][2], acc[c][q][3])};
        st8(o, rs, lane * 8, OFF_BF + (unsigned)((q & 1) * 28 + 4 * wave + c) * 512u + (unsigned)(2 * sp + (q >> 1)) * BF_S);
      }
  }
  VMWAIT0();
  __syncthreads();
  if (tid == 0) st4(1u, rs, FL_F2 + (unsigned)sp * FS, 0);
}

template <int Q>
__device__ __forceinline__ void attn_softmax(const f32x4 sm, const f32x4 ta, const f32x4 tb, int g, int n, u32x4& pt, u32x2& pm) {
  const float NEG = -3.0e38f;
  float t[8];
#pragma unroll
  for (int k = 0; k < 4; ++k) {
    const bool causal = (4 * g + k) <= n;
    t[k] = Q ? ta[k] : (causal ? ta[k] : NEG);
    t[4 + k] = Q ? (causal ? tb[k] : NEG) : NEG;
  }
  float m = vmax(vmax(vmax(sm[0], sm[1]), vmax(sm[2], sm[3])), vmax(vmax(t[0], t[1]), vmax(t[2], t[3])));
  if (Q) m = vmax(m, vmax(vmax(t[4], t[5]), vmax(t[6], t[7])));
  m = max32(max16(m));
  float e[4], l = 0.f;
#pragma unroll
  for (int k = 0; k < 4; ++k) { e[k] = __builtin_amdgcn_exp2f(sm[k] - m); l += e[k]; }
  l *= 0.5f;
#pragma unroll
  for (int k = 0; k < (Q ? 8 : 4); ++k) { t[k] = __builtin_amdgcn_exp2f(t[k] - m); l += t[k]; }
  l = sum32(sum16(l));
  const float rl = __builtin_amdgcn_rcpf(l);
  pt = u32x4{pk(t[0] * rl, t[1] * rl), pk(t[2] * rl, t[3] * rl), Q ? pk(t[4] * rl, t[5] * rl) : 0u, Q ? pk(t[6] * rl, t[7] * rl) : 0u};
  pm = u32x2{pk(e[0] * rl, e[1] * rl), pk(e[2] * rl, e[3] * rl)};
}

__device__ __forceinline__ void task_attn(const Params& P, int s, int cbg, LAS unsigned char* lds, int& tmo) {
  int tid = threadIdx.x;
  asm volatile("" : "+v"(tid));
  const int lane = tid & 63, wave = __builtin_amdgcn_readfirstlane(tid >> 6);
  const rsrc_t rs = ws_rsrc(P.ws);
  const int n = lane & 15, g = lane >> 4;
  LAS unsigned char* PT = lds + 8192;
  LAS unsigned char* PM = PT + 32768;
  LAS unsigned char* VS = PM + 16384;
  __syncthreads();
  {
    const unsigned fo = lane < 32 ? FL_T1V + (unsigned)lane * FS : (lane & 1) ? FL_T2 + (unsigned)s * FS : FL_F2 + (unsigned)(s >> 1) * FS;
    if (wave == 0) wait_lanes<24>(rs, fo, lane < 32 ? s + 1 : 1, tmo);
    BAR();
  }
  const int cbl = wave >> 1, hh8 = wave & 1, cb = 4 * cbg + cbl, qo = hh8;
  f32x4 sxx[2][3];
  u32x2 bf;
  float gate;
  {
    u32x4 vx[8];
#pragma unroll
    for (int k = 0; k < 8; ++k) vx[k] = ld16(rs, lane * 16, OFF_VX + (unsigned)((2 * wave + (k >> 2)) * 28 + 4 * cbg + (k & 3)) * 1024u + (unsigned)s * VX_S);
#pragma unroll
    for (int hh = 0; hh < 2; ++hh)
#pragma unroll
      for (int k = 0; k < 3; ++k) sxx[hh][k] = __builtin_bit_cast(f32x4, ld16(rs, lane * 16, OFF_SXX + (unsigned)((2 * wave + hh) * 3 + k) * 1024u + (unsigned)s * SXX_S));
    bf = ld8(rs, lane * 8, OFF_BF + (unsigned)(qo * 28 + cb) * 512u + (unsigned)s * BF_S);
    gate = __builtin_bit_cast(float, ld4(rs, (unsigned)n * 4u, OFF_G + (unsigned)(32 * s + 16 * qo) * 4u));
#pragma unroll
    for (int k = 0; k < 8; ++k) *(LAS u32x4*)(VS + ((2 * wave + (k >> 2)) * 4 + (k & 3)) * 1024 + lane * 16) = vx[k];
  }
  VMWAIT0();
  __syncthreads();
  if (wave == 0) {
    const unsigned vo = FL_CH + (unsigned)(lane < 60 ? lane : 0) * FS;
    for (unsigned sp = 0;; ++sp) {
      asm volatile("" ::: "memory");
      const int v = (int)ld4(rs, vo, 0);
      if (__builtin_amdgcn_ballot_w64(v < s + 1) == 0ull) break;
      if (sp > SPIN_MAX || tmo) { tmo = 1; break; }
      const int v0 = __builtin_amdgcn_readfirstlane(v);
      const int far = min(s - v0 - 1, 16);
      for (int k = 0; k < far; ++k) __builtin_amdgcn_s_sleep(48);
      __builtin_amdgcn_s_sleep(4);
    }
  }
  BAR();
  f32x4 smq[2][2];
  u32x2 vm[8];
  {
    u32x4 sp[2][2][2];
#pragma unroll
    for (int hh = 0; hh < 2; ++hh)
#pragma unroll
      for (int q = 0; q < 2; ++q)
#pragma unroll
        for (int hq = 0; hq < 2; ++hq)
          sp[hh][q][hq] = ld16(rs, (unsigned)((g & 1) * 16 + n) * 16u, OFF_KM + (unsigned)((2 * wave + hh) * 2 + hq) * 1024u + (unsigned)q * 512u + (unsigned)s * KM_S);
#pragma unroll
    for (int h = 0; h < 8; ++h) vm[h] = ld8(rs, lane * 8, OFF_VM + (unsigned)((8 * hh8 + h) * 28 + cb) * 512u + (unsigned)s * VM_S);
    SCHED();
#pragma unroll
    for (int hh = 0; hh < 2; ++hh)
#pragma unroll
      for (int q = 0; q < 2; ++q) smq[hh][q] = __builtin_bit_cast(f32x4, sp[hh][q][0]) + __builtin_bit_cast(f32x4, sp[hh][q][1]);
  }
#pragma unroll
  for (int hh = 0; hh < 2; ++hh) {
    const int h = 2 * wave + hh;
    u32x4 pt;
    u32x2 pm;
    attn_softmax<0>(smq[hh][0], sxx[hh][0], sxx[hh][2], g, n, pt, pm);
    *(LAS u32x4*)(PT + (h * 2 + 0) * 1024 + lane * 16) = pt;
    *(LAS u32x2*)(PM + (h * 2 + 0) * 512 + lane * 8) = pm;
    attn_softmax<1>(smq[hh][1], sxx[hh][1], sxx[hh][2], g, n, pt, pm);
    *(LAS u32x4*)(PT + (h * 2 + 1) * 1024 + lane * 16) = pt;
    *(LAS u32x2*)(PM + (h * 2 + 1) * 512 + lane * 8) = pm;
  }
  __syncthreads();
  f32x4 at[2] = {{0, 0, 0, 0}, {0, 0, 0, 0}}, am[2] = {{0, 0, 0, 0}, {0, 0, 0, 0}};
#pragma unroll
  for (int k = 0; k < 8; ++k) {
    const int h = 8 * hh8 + k;
    const bf16x8 vx = *(const LAS bf16x8*)(VS + (h * 4 + cbl) * 1024 + lane * 16);
#pragma unroll
    for (int q = 0; q < 2; ++q) {
      const bf16x8 pt = *(const LAS bf16x8*)(PT + (h * 2 + q) * 1024 + lane * 16);
      const u32x2 pm = *(const LAS u32x2*)(PM + (h * 2 + q) * 512 + lane * 8);
      at[q] = mfma32(vx, pt, at[q]);
      am[q] = mfma16(as_s4(vm[k]), as_s4(pm), am[q]);
    }
  }
  LAS f32x4* ex = (LAS f32x4*)lds;
  ex[(wave ^ 1) * 64 + lane] = hh8 ? at[0] + am[0] : at[1] + am[1];
  __syncthreads();
  const f32x4 acc = (hh8 ? at[1] + am[1] : at[0] + am[0]) + ex[wave * 64 + lane];
  const float gb = bflo(pk(gate, 0.f));
  const float og = bflo(pk(1.0f - gb, 0.f));
  const float bv[4] = {bflo(bf.x), bfhi(bf.x), bflo(bf.y), bfhi(bf.y)};
  float o[4];
#pragma unroll
  for (int k = 0; k < 4; ++k) {
    const float av = bflo(pk(acc[k], 0.f));
    o[k] = bflo(pk(gb * av, 0.f)) + bflo(pk(og * bv[k], 0.f));
  }
  const u32x2 ov = {pk(o[0], o[1]), pk(o[2], o[3])};
  *(u32x2*)(P.out + (size_t)(32 * s + 16 * qo + n) * DM + 16 * cb + 4 * g) = ov;
}

constexpr int NTASK = 32 * 17;
__device__ __forceinline__ void generic_wg(const Params& P, LAS unsigned char* lds) {
  int tmo = 0;
  LAS int* tk = (LAS int*)(lds + LDS_BYTES - 16);
  for (;;) {
    __syncthreads();
    if (threadIdx.x == 0) *tk = (int)__hip_atomic_fetch_add((GAS unsigned*)(P.ws + FL_Q), 1u, __ATOMIC_RELAXED, __HIP_MEMORY_SCOPE_AGENT);
    __syncthreads();
    const int t = __builtin_amdgcn_readfirstlane(*tk);
    if (t >= NTASK) break;
    int kind = 2, s = 0, c = 0;
    {
      int cnt = t, npre = 0, p = -1;
      for (;;) {
        const int lim = min(32, p < 0 ? 2 : (3 * (p + 1)) / 2 + 2);
        if (cnt < 3 * (lim - npre)) { const int pp = npre + cnt / 3, o = cnt % 3; kind = o == 0 ? 1 : 0; s = o == 0 ? pp : 2 * pp + o - 1; break; }
        cnt -= 3 * (lim - npre); npre = lim; ++p;
        if (cnt < 14) { kind = 2; s = 2 * p + (cnt >= 7 ? 1 : 0); c = cnt >= 7 ? cnt - 7 : cnt; break; }
        cnt -= 14;
      }
    }
    if (kind == 0) task_sxx(P, s, lds, tmo);
    else if (kind == 1) task_ffn2(P, s, lds, tmo);
    else task_attn(P, s, c, lds, tmo);
  }
}

__global__ void __launch_bounds__(512, 2) main_kernel(Params P) {
  extern __shared__ __attribute__((aligned(16))) unsigned char smem[];
  LAS unsigned char* lds = (LAS unsigned char*)smem;
  const int b = blockIdx.x, r = b >> 3;
  const int x = (b + (int)*(const unsigned*)(P.ws + FL_ROT)) & 7;
  if (x < 4) {
    if (x == 0 || x == 2) {
      const int idx = (x >> 1) * 16 + (r & 15);
      if (r < 16) {
        const int wave = __builtin_amdgcn_readfirstlane(threadIdx.x >> 6);
        if (wave < 7) score_main(P, idx, wave, lds); else score_sx(P, idx, lds);
      }
    } else if (r < 14) {
      value_wg(P, (x >> 1) * 14 + r, lds);
    } else if (x == 1 && r == 31) probe_pol(P);
  } else {
    const int y = x - 4;
    if (r < 8) stream_wg<0>(P, y * 8 + r, lds);
    else if (r < 16) stream_wg<1>(P, y * 8 + r - 8, lds);
    else if (r < 20) stream_wg<2>(P, y * 4 + r - 16, lds);
    generic_wg(P, lds);
  }
}

}

extern "C" void kernel_launch(void* const* d_in, const int* in_sizes, int n_in, void* d_out, int out_size, void* d_ws, size_t ws_size,
                              hipStream_t stream) {
  Params P;
  P.x = (const unsigned short*)d_in[0];
  P.mem0 = (const unsigned short*)d_in[1];
  P.Wq = (const unsigned short*)d_in[2];
  P.Wk = (const unsigned short*)d_in[3];
  P.Wv = (const unsigned short*)d_in[4];
  P.Wo = (const unsigned short*)d_in[5];
  P.Wg = (const unsigned short*)d_in[8];
  P.Wu = (const unsigned short*)d_in[9];
  P.Wd = (const unsigned short*)d_in[10];
  P.gw = (const unsigned short*)d_in[11];
  P.out = (unsigned short*)d_out;
  const uintptr_t base = ((uintptr_t)d_ws + ((1u << 21) - 1)) & ~(uintptr_t)((1u << 21) - 1);
  P.ws = (unsigned char*)base;
  (void)hipFuncSetAttribute((const void*)prep_kernel, hipFuncAttributeMaxDynamicSharedMemorySize, 57344);
  (void)hipFuncSetAttribute((const void*)main_kernel, hipFuncAttributeMaxDynamicSharedMemorySize, LDS_BYTES);
  hipLaunchKernelGGL(prep_kernel, dim3(256), dim3(512), 57344, stream, P);
  hipLaunchKernelGGL(main_kernel, dim3(256), dim3(512), LDS_BYTES, stream, P);
}
```
